```python
import torch

def _softmax(x, axis=-1):
    return torch.softmax(x, dim=axis)
def _gelu(x):
    return torch.nn.functional.gelu(x, approximate="tanh")
def _silu(x):
    return torch.nn.functional.silu(x)
def _sigmoid(x):
    return torch.sigmoid(x)
def _layernorm(x, axis=-1, eps=1e-5):
    m = x.mean(dim=axis, keepdim=True)
    v = x.var(dim=axis, keepdim=True, unbiased=False)
    return (x - m) / torch.sqrt(v + eps)
def _scatter_add(tgt, idx, src, axis=0):
    return tgt.clone().index_add_(axis, idx, src)
def _segment_sum(src, seg, n):
    out = torch.zeros((n,) + src.shape[1:], dtype=src.dtype)
    return out.index_add_(0, seg, src)
def _pad(x, pw):
    flat = tuple(v for pair in reversed(pw) for v in pair)
    return torch.nn.functional.pad(x, flat)

kernel_name = 'parallel_sum__spectral_fnet__dense_activation_relu__prompt'

def setup_inputs():

    _w = (lambda a: (lambda t: t.to(torch.bfloat16) if t.is_floating_point() else t)(torch.as_tensor(a)))
    def _A_setup():
        B, S, D = 1, 2048, 512
        ns = torch.arange(S)[:, None] * torch.arange(S)[None, :]
        nd = torch.arange(D)[:, None] * torch.arange(D)[None, :]
        Ws_re = torch.cos(-2.0 * torch.pi * ns / S).to(float)
        Ws_im = torch.sin(-2.0 * torch.pi * ns / S).to(float)
        Wd_re = torch.cos(-2.0 * torch.pi * nd / D).to(float)
        Wd_im = torch.sin(-2.0 * torch.pi * nd / D).to(float)
        return {
            "x": _w((lambda *s: torch.randn(*s, dtype=torch.bfloat16) * 0.05)(B, S, D)),
            "dft_seq_re": _w(Ws_re),
            "dft_seq_im": _w(Ws_im),
            "dft_hid_re": _w(Wd_re),
            "dft_hid_im": _w(Wd_im),
        }
    _A_in = _A_setup()

    def _B_setup():
        B, S, D, Dff = 1, 2048, 512, 1792
        return {
            "x": _w((lambda *s: torch.randn(*s, dtype=torch.bfloat16) * 0.05)(B, S, D)),
            "w1": _w((lambda *s: torch.randn(*s, dtype=torch.bfloat16) * 0.4)(D, Dff)),
            "w2": _w((lambda *s: torch.randn(*s, dtype=torch.bfloat16) * 0.4)(Dff, D)),
        }
    _B_in = _B_setup()

    return {
        "A_x": _A_in["x"],
        "A_dft_seq_re": _A_in["dft_seq_re"],
        "A_dft_seq_im": _A_in["dft_seq_im"],
        "A_dft_hid_re": _A_in["dft_hid_re"],
        "A_dft_hid_im": _A_in["dft_hid_im"],
        "B_x": _B_in["x"],
        "B_w1": _B_in["w1"],
        "B_w2": _B_in["w2"],
    }

def reference(A_x, A_dft_seq_re, A_dft_seq_im, A_dft_hid_re, A_dft_hid_im, B_x, B_w1, B_w2):
    def _A_ref(x, dft_seq_re, dft_seq_im, dft_hid_re, dft_hid_im):
        a = torch.einsum("bsd,ed->bse", x, dft_hid_re)
        b = torch.einsum("bsd,ed->bse", x, dft_hid_im)
        y = torch.einsum("ts,bse->bte", dft_seq_re, a) - torch.einsum("ts,bse->bte", dft_seq_im, b)
        return y

    def _B_ref(x, w1, w2):
        h = torch.einsum("bsd,df->bsf", x, w1)
        zero = torch.zeros_like(h)
        h = torch.maximum(h, zero)
        return torch.einsum("bsf,fd->bsd", h, w2)

    x = A_x
    xn = _layernorm(x, axis=-1)
    return x + _A_ref(xn, A_dft_seq_re, A_dft_seq_im, A_dft_hid_re, A_dft_hid_im) + _B_ref(xn, B_w1, B_w2)
```

```cpp
#include <hip/hip_runtime.h>
#include <hip/hip_bf16.h>
#include <stdint.h>

typedef __attribute__((ext_vector_type(8))) short bf16x8;
typedef __attribute__((ext_vector_type(4))) short s16x4;
typedef __attribute__((ext_vector_type(4))) float f32x4;
typedef __attribute__((ext_vector_type(4))) unsigned u32x4;
typedef __attribute__((ext_vector_type(2))) unsigned u32x2;
typedef unsigned short u16;

#define LDS_AS __attribute__((address_space(3)))
#define WAIT_V(n) asm volatile("s_waitcnt vmcnt(%0)" ::"n"(n) : "memory")
#define WAIT_L0() asm volatile("s_waitcnt lgkmcnt(0)" ::: "memory")
#define BAR() do { asm volatile("" ::: "memory"); __builtin_amdgcn_s_barrier(); asm volatile("" ::: "memory"); } while (0)

constexpr int S_ = 2048, D_ = 512, DFF = 1792;
constexpr int HLD = 2048;

constexpr size_t WS_AB = 0;
constexpr size_t WS_H = WS_AB + (size_t)4096 * 512 * 2;
constexpr size_t WS_P = WS_H + (size_t)2048 * HLD * 2;

__device__ __forceinline__ float bf16_round(float f) {
  return __bfloat162float(__float2bfloat16(f));
}
__device__ __forceinline__ unsigned pack_bf16(float lo, float hi) {
  __hip_bfloat16 a = __float2bfloat16(lo), b = __float2bfloat16(hi);
  return (unsigned)__builtin_bit_cast(u16, a) | ((unsigned)__builtin_bit_cast(u16, b) << 16);
}
__device__ __forceinline__ void glds16(const void* g, char* l) {
  __builtin_amdgcn_global_load_lds((const unsigned*)g, (unsigned*)l, 16, 0, 0);
}
__device__ __forceinline__ s16x4 tr_read(const char* p) {
  return __builtin_amdgcn_ds_read_tr16_b64_v4i16((LDS_AS s16x4*)(p));
}

constexpr int K1_STAGE = 49152;

template <bool NC>
__device__ __forceinline__ void k1_body(char* smem, const u16* __restrict__ x, const u16* __restrict__ wsrc,
                                        u16* __restrict__ dst, int dst_ld, int m0, int col0, int st_lo, int st_hi,
                                        float sgn) {
  constexpr int NG = NC ? 12 : 11;
  const int tid = threadIdx.x, lane = tid & 63;
  const int wid = __builtin_amdgcn_readfirstlane(tid >> 6);
  const int r = lane & 15, g = lane >> 4;

  u32x4 xf[2][16];
  {
    const u16* p0 = x + (size_t)(m0 + wid * 32 + r) * D_ + g * 8;
    const u16* p1 = p0 + 16 * D_;
#pragma unroll
    for (int kk = 0; kk < 16; ++kk) {
      asm volatile("global_load_dwordx4 %0, %1, off offset:%2" : "=v"(xf[0][kk]) : "v"(p0), "n"(kk * 64) : "memory");
      asm volatile("global_load_dwordx4 %0, %1, off offset:%2" : "=v"(xf[1][kk]) : "v"(p1), "n"(kk * 64) : "memory");
    }
  }
  int soff[6];
  if constexpr (!NC) {
    const int rr = 4 * wid + g;
    soff[0] = (rr * D_ + ((r ^ rr) * 8)) * 2;
  } else {
#pragma unroll
    for (int v = 0; v < 3; ++v) {
      const int idx = 64 * v + lane, kr = idx / 24, c = idx % 24;
      const int blk = c >> 3, o = (c >> 1) & 3, hf = c & 1;
#pragma unroll
      for (int up = 0; up < 2; ++up) {
        const int X = ((kr >> 1) & 1) | (up << 1);
        soff[v * 2 + up] = ((32 * wid + kr) * DFF + blk * 64 + (o ^ X) * 16 + hf * 8) * 2;
      }
    }
  }
  const char* wbase = NC ? (const char*)(wsrc + col0) : (const char*)(wsrc + (size_t)col0 * D_);
  auto stage = [&](int st, int buf) {
    char* lb = smem + buf * K1_STAGE;
    if constexpr (!NC) {
#pragma unroll
      for (int j = 0; j < NG; ++j)
        glds16(wbase + soff[0] + j * 16 * D_ * 2 + st * 256, lb + j * 4096 + wid * 1024);
    } else {
#pragma unroll
      for (int u = 0; u < 4; ++u)
#pragma unroll
        for (int v = 0; v < 3; ++v)
          glds16(wbase + soff[v * 2 + (u & 1)] + (size_t)(st * 128 + 8 * u) * DFF * 2,
                 lb + (12 * wid + 3 * u + v) * 1024);
    }
  };
  stage(0, 0);
  stage(1, 1);
  stage(2, 2);
  WAIT_V(3 * NG);
#pragma unroll
  for (int kk = 0; kk < 16; ++kk) asm volatile("" : "+v"(xf[0][kk]), "+v"(xf[1][kk]));

  float mb[2], md[2];
  {
    bf16x8 ones;
#pragma unroll
    for (int i = 0; i < 8; ++i) ones[i] = (short)0x3F80;
#pragma unroll
    for (int rg = 0; rg < 2; ++rg) {
      f32x4 s = {0.f, 0.f, 0.f, 0.f};
#pragma unroll
      for (int kk = 0; kk < 16; ++kk)
        s = __builtin_amdgcn_mfma_f32_16x16x32_bf16(ones, __builtin_bit_cast(bf16x8, xf[rg][kk]), s, 0, 0, 0);
      const float mean = s[0] * (1.0f / 512.0f);
      mb[rg] = bf16_round(mean);
      md[rg] = mean - mb[rg];
    }
  }

  f32x4 acc[2][NG];
#pragma unroll
  for (int rg = 0; rg < 2; ++rg)
#pragma unroll
    for (int j = 0; j < NG; ++j) acc[rg][j] = f32x4{0.f, 0.f, 0.f, 0.f};
  f32x4 sq[2] = {f32x4{0.f, 0.f, 0.f, 0.f}, f32x4{0.f, 0.f, 0.f, 0.f}};

  int roff[4];
  if constexpr (!NC) {
#pragma unroll
    for (int ks = 0; ks < 4; ++ks) roff[ks] = r * 256 + (((4 * ks + g) ^ r) * 16);
  } else {
    const int q = r >> 2, p = r & 3;
    const int X = ((q >> 1) & 1) | ((g & 1) << 1);
#pragma unroll
    for (int jj = 0; jj < 4; ++jj) roff[jj] = (8 * g + q) * 384 + ((jj ^ X) * 32) + p * 8;
  }

#pragma unroll
  for (int st = 0; st < 4; ++st) {
    if (st == 0) WAIT_V(2 * NG);
    else if (st == 1) WAIT_V(2 * NG);
    else if (st == 2) WAIT_V(NG);
    else WAIT_V(0);
    BAR();
    const char* lb = smem + (st % 3) * K1_STAGE;
#pragma unroll
    for (int ks = 0; ks < 4; ++ks) {
      const int kk = st * 4 + ks;
      bf16x8 df[2];
#pragma unroll
      for (int rg = 0; rg < 2; ++rg) {
        u32x4 o;
#pragma unroll
        for (int i = 0; i < 4; ++i) {
          const unsigned u = xf[rg][kk][i];
          const float lo = __builtin_bit_cast(float, u << 16) - mb[rg];
          const float hi = __builtin_bit_cast(float, u & 0xffff0000u) - mb[rg];
          o[i] = pack_bf16(lo, hi);
        }
        df[rg] = __builtin_bit_cast(bf16x8, o);
        sq[rg] = __builtin_amdgcn_mfma_f32_16x16x32_bf16(df[rg], df[rg], sq[rg], 0, 0, 0);
      }
#pragma unroll
      for (int j = 0; j < NG; ++j) {
        bf16x8 wf;
        if constexpr (!NC) {
          wf = *(const bf16x8*)(lb + j * 4096 + roff[ks]);
        } else {
          const char* p = lb + roff[j & 3] + (32 * ks) * 384 + (j >> 2) * 128;
          s16x4 t0 = tr_read(p), t1 = tr_read(p + 4 * 384);
          wf = __builtin_shufflevector(t0, t1, 0, 1, 2, 3, 4, 5, 6, 7);
        }
        acc[0][j] = __builtin_amdgcn_mfma_f32_16x16x32_bf16(wf, df[0], acc[0][j], 0, 0, 0);
        acc[1][j] = __builtin_amdgcn_mfma_f32_16x16x32_bf16(wf, df[1], acc[1][j], 0, 0, 0);
      }
    }
    if (st == 0) {
      WAIT_L0();
      BAR();
      stage(3, 0);
    }
  }

#pragma unroll
  for (int rg = 0; rg < 2; ++rg) {
    const int sel = lane & 3;
    float dv = sel == 0 ? sq[rg][0] : sel == 1 ? sq[rg][1] : sel == 2 ? sq[rg][2] : sq[rg][3];
    dv = __shfl(dv, r + 16 * (r >> 2), 64);
    float var = dv * (1.0f / 512.0f) - md[rg] * md[rg];
    var = fmaxf(var, 0.f);
    const float vb = bf16_round(var);
    const float den = bf16_round(sqrtf(bf16_round(vb + 1e-5f)));
    const float rs = sgn / den;
    u16* orow = dst + (size_t)(m0 + wid * 32 + rg * 16 + r) * dst_ld + col0 + 4 * g;
#pragma unroll
    for (int j = 0; j < NG; ++j) {
      if (j >= st_lo && j < st_hi) {
        float v0 = acc[rg][j][0] * rs, v1 = acc[rg][j][1] * rs, v2 = acc[rg][j][2] * rs, v3 = acc[rg][j][3] * rs;
        if constexpr (NC) {
          v0 = fmaxf(v0, 0.f); v1 = fmaxf(v1, 0.f); v2 = fmaxf(v2, 0.f); v3 = fmaxf(v3, 0.f);
        }
        u32x2 o;
        o[0] = pack_bf16(v0, v1);
        o[1] = pack_bf16(v2, v3);
        *(u32x2*)(orow + j * 16) = o;
      }
    }
  }
}

__global__ __launch_bounds__(256, 1) void k1_ln_gemm1(const u16* __restrict__ x, const u16* __restrict__ wdre,
                                                      const u16* __restrict__ wdim, const u16* __restrict__ w1,
                                                      u16* __restrict__ ab, u16* __restrict__ h) {
  __shared__ __attribute__((aligned(1024))) char smem[3 * K1_STAGE];
  const int bid = blockIdx.x, xcd = bid & 7, idx = bid >> 3;
  const int mt = 8 * (xcd & 1) + (idx >> 2), ntile = 4 * (xcd >> 1) + (idx & 3);
  const int m0 = mt * 128;
  if (ntile < 6) {
    const int im = ntile >= 3, t = ntile - 3 * im;
    const int g0 = t == 2 ? 21 : 11 * t;
    k1_body<false>(smem, x, im ? wdim : wdre, ab + (size_t)im * 2048 * D_, D_, m0, g0 * 16, t == 2 ? 1 : 0, 11,
                   im ? -1.f : 1.f);
  } else {
    const int t = ntile - 6;
    const int s0 = (t * 112) / 10, s1 = ((t + 1) * 112) / 10;
    const int g0 = s0 < 100 ? s0 : 100;
    k1_body<true>(smem, x, w1, h, HLD, m0, g0 * 16, s0 - g0, s1 - g0, 1.f);
  }
}

constexpr int K2_NT = 23, K2_STAGE = 32768, K2_NBUF = 4;

__global__ __launch_bounds__(512, 2) void k2_gemm2(const u16* __restrict__ wsre, const u16* __restrict__ wsim,
                                                   const u16* __restrict__ h, const u16* __restrict__ ab,
                                                   const u16* __restrict__ w2, float* __restrict__ part) {
  __shared__ __attribute__((aligned(1024))) char smem[K2_NBUF * K2_STAGE];
  const int tid = threadIdx.x, lane = tid & 63;
  const int wid = __builtin_amdgcn_readfirstlane(tid >> 6);
  const int bid = blockIdx.x, xcd = bid & 7, idx = bid >> 3;
  const int ksl = xcd >> 1, mt = 8 * (xcd & 1) + (idx >> 2), nt = idx & 3;
  const int m0 = mt * 128, n0 = nt * 128;
  const int r = lane & 15, g = lane >> 4;

  if (wid >= 4) {
    const int lw = wid - 4;
    int aoff[4], boff[4];
#pragma unroll
    for (int i = 0; i < 4; ++i) {
      const int row = 32 * lw + 8 * i + (lane >> 3);
      aoff[i] = (m0 + row) * 4096 + (((lane & 7) ^ ((row >> 1) & 7)) * 16);
      const int kr = 16 * lw + 4 * i + g;
      const int X = ((kr & 3) << 2) | ((kr >> 2) & 3);
      boff[i] = kr * 1024 + n0 * 2 + ((r ^ X) * 16);
    }
    auto stage = [&](int t) {
      const int gs = ksl * K2_NT + t;
      const char* pa = gs < 32 ? (const char*)wsre + gs * 128
                     : gs < 64 ? (const char*)wsim + (gs - 32) * 128 : (const char*)h + (gs - 64) * 128;
      const char* pb = gs < 64 ? (const char*)ab + (size_t)gs * 65536 : (const char*)w2 + (size_t)(gs - 64) * 65536;
      char* lb = smem + (t & 3) * K2_STAGE + lw * 4096;
#pragma unroll
      for (int i = 0; i < 4; ++i) glds16(pa + aoff[i], lb + i * 1024);
#pragma unroll
      for (int i = 0; i < 4; ++i) glds16(pb + boff[i], lb + 16384 + i * 1024);
    };
    stage(0);
    stage(1);
    stage(2);
    for (int t = 0; t < K2_NT - 3; ++t) {
      WAIT_V(16);
      BAR();
      stage(t + 3);
    }
    WAIT_V(16);
    BAR();
    WAIT_V(8);
    BAR();
    WAIT_V(0);
    BAR();
    return;
  }
  const int wr = wid >> 1, wc = wid & 1;
  int aoff[2], boff[4][2];
#pragma unroll
  for (int ks = 0; ks < 2; ++ks) aoff[ks] = (64 * wr + r) * 128 + (((4 * ks + g) ^ (r >> 1)) * 16);
  {
    const int q = r >> 2, p = r & 3;
#pragma unroll
    for (int j = 0; j < 4; ++j)
#pragma unroll
      for (int t = 0; t < 2; ++t) {
        const int X = (q << 2) | ((2 * g + t) & 3);
        const int ch = 8 * wc + 2 * j + (p >> 1);
        boff[j][t] = 16384 + (8 * g + 4 * t + q) * 256 + ((ch ^ X) * 16) + 8 * (p & 1);
      }
  }
  f32x4 acc[4][4];
#pragma unroll
  for (int i = 0; i < 4; ++i)
#pragma unroll
    for (int j = 0; j < 4; ++j) acc[i][j] = f32x4{0.f, 0.f, 0.f, 0.f};

  for (int t = 0; t < K2_NT; ++t) {
    BAR();
    const char* lb = smem + (t & 3) * K2_STAGE;
#pragma unroll
    for (int ks = 0; ks < 2; ++ks) {
      bf16x8 am[4], bm[4];
#pragma unroll
      for (int i = 0; i < 4; ++i) am[i] = *(const bf16x8*)(lb + aoff[ks] + i * 2048);
#pragma unroll
      for (int j = 0; j < 4; ++j) {
        s16x4 t0 = tr_read(lb + boff[j][0] + ks * 8192), t1 = tr_read(lb + boff[j][1] + ks * 8192);
        bm[j] = __builtin_shufflevector(t0, t1, 0, 1, 2, 3, 4, 5, 6, 7);
      }
#pragma unroll
      for (int i = 0; i < 4; ++i)
#pragma unroll
        for (int j = 0; j < 4; ++j)
          acc[i][j] = __builtin_amdgcn_mfma_f32_16x16x32_bf16(bm[j], am[i], acc[i][j], 0, 0, 0);
    }
    WAIT_L0();
  }
  float* pp = part + (size_t)ksl * S_ * D_ + (size_t)(m0 + 64 * wr + r) * D_ + n0 + 64 * wc + 4 * g;
#pragma unroll
  for (int i = 0; i < 4; ++i)
#pragma unroll
    for (int j = 0; j < 4; ++j) *(f32x4*)(pp + i * 16 * D_ + j * 16) = acc[i][j];
}

__global__ __launch_bounds__(256) void k3_reduce(const float* __restrict__ part, const u16* __restrict__ x,
                                                 u16* __restrict__ out) {
  const size_t i = ((size_t)blockIdx.x * 256 + threadIdx.x) * 8;
  f32x4 s0 = *(const f32x4*)(part + i), s1 = *(const f32x4*)(part + i + 4);
#pragma unroll
  for (int k = 1; k < 4; ++k) {
    s0 += *(const f32x4*)(part + (size_t)k * S_ * D_ + i);
    s1 += *(const f32x4*)(part + (size_t)k * S_ * D_ + i + 4);
  }
  const u32x4 xv = *(const u32x4*)(x + i);
  u32x4 o;
  o[0] = pack_bf16(s0[0] + __builtin_bit_cast(float, xv[0] << 16), s0[1] + __builtin_bit_cast(float, xv[0] & 0xffff0000u));
  o[1] = pack_bf16(s0[2] + __builtin_bit_cast(float, xv[1] << 16), s0[3] + __builtin_bit_cast(float, xv[1] & 0xffff0000u));
  o[2] = pack_bf16(s1[0] + __builtin_bit_cast(float, xv[2] << 16), s1[1] + __builtin_bit_cast(float, xv[2] & 0xffff0000u));
  o[3] = pack_bf16(s1[2] + __builtin_bit_cast(float, xv[3] << 16), s1[3] + __builtin_bit_cast(float, xv[3] & 0xffff0000u));
  *(u32x4*)(out + i) = o;
}

extern "C" void kernel_launch(void* const* d_in, const int* in_sizes, int n_in, void* d_out, int out_size,
                              void* d_ws, size_t ws_size, hipStream_t stream) {
  const u16* x = (const u16*)d_in[0];
  const u16* wsre = (const u16*)d_in[1];
  const u16* wsim = (const u16*)d_in[2];
  const u16* wdre = (const u16*)d_in[3];
  const u16* wdim = (const u16*)d_in[4];
  const u16* w1 = (const u16*)d_in[6];
  const u16* w2 = (const u16*)d_in[7];
  char* ws = (char*)d_ws;
  u16* ab = (u16*)(ws + WS_AB);
  u16* h = (u16*)(ws + WS_H);
  float* part = (float*)(ws + WS_P);
  k1_ln_gemm1<<<256, 256, 0, stream>>>(x, wdre, wdim, w1, ab, h);
  k2_gemm2<<<256, 512, 0, stream>>>(wsre, wsim, h, ab, w2, part);
  k3_reduce<<<S_ * D_ / 8 / 256, 256, 0, stream>>>(part, x, (u16*)d_out);
}
```

```cpp
#include <hip/hip_runtime.h>
#include <hip/hip_bf16.h>
#include <stdint.h>

typedef __attribute__((ext_vector_type(8))) short bf16x8;
typedef __attribute__((ext_vector_type(4))) short s16x4;
typedef __attribute__((ext_vector_type(2))) short s16x2;
typedef __attribute__((ext_vector_type(4))) float f32x4;
typedef __attribute__((ext_vector_type(2))) float f32x2;
typedef __attribute__((ext_vector_type(2))) __bf16 bf16x2_t;
typedef __attribute__((ext_vector_type(4))) unsigned u32x4;
typedef __attribute__((ext_vector_type(2))) unsigned u32x2;
typedef unsigned short u16;

#define LDS_AS __attribute__((address_space(3)))
#define GLB_AS __attribute__((address_space(1)))
#define WAIT_V(n) asm volatile("s_waitcnt vmcnt(%0)" ::"n"(n) : "memory")
#define WAIT_L0() asm volatile("s_waitcnt lgkmcnt(0)" ::: "memory")
#define WAIT_L(n) __builtin_amdgcn_s_waitcnt(0xC07F | ((n) << 8))
#define BAR() do { asm volatile("" ::: "memory"); __builtin_amdgcn_s_barrier(); asm volatile("" ::: "memory"); } while (0)
#define SCHED() __builtin_amdgcn_sched_barrier(0)

#define STAMP_DECL
#define STAMP(i)
#define RSTAMP(i)
#define STAMP_FLUSH(k)

#define BLK_LAYOUT 1
constexpr int S_ = 2048, D_ = 512, DFF = 1792;
constexpr int HLD = DFF;

constexpr size_t WS_CNT = 0;
constexpr size_t WS_AB = 65536;
constexpr size_t WS_H = WS_AB + (size_t)4096 * 512 * 2;
constexpr size_t WS_P = WS_H + (size_t)2048 * 2048 * 2;

#define TSL 7, 6
#define NA 5
#define SIG_T 1
#define POLL_T 3
#define POLL_D 1
#define XL_T (POLL_T - 2)
#define ROT1 0
#define ROT2 2
#define ROT3 1
#define CPF1 1
#define CPF2 1
#define TPAT 2
#define ST0 (2 + (idx % 5))
#define ST1 7
#define PGT 1
#define TPOL ""
#define TOUCH64(d, p) asm volatile("global_load_dword %0, %1, off" TPOL : "+v"(d) : "v"(p) : "memory")
#define TOUCH(d, off, base) asm volatile("global_load_dword %0, %1, %2" : "+v"(d) : "v"(off), "s"(base) : "memory")

__device__ __forceinline__ void wait_v(int n) {
  switch (n) {
#define WV(k) case k: WAIT_V(k); break;
    WV(0) WV(1) WV(2) WV(3) WV(4) WV(5) WV(6) WV(10) WV(11) WV(12) WV(13) WV(14) WV(15) WV(16)
#undef WV
    default: WAIT_V(0);
  }
}
__device__ __forceinline__ float bf16_round(float f) { return __bfloat162float(__float2bfloat16(f)); }
__device__ __forceinline__ unsigned pack_bf16(float lo, float hi) {
  f32x2 v = {lo, hi};
  return __builtin_bit_cast(unsigned, __builtin_convertvector(v, bf16x2_t));
}
__device__ __forceinline__ float bf_lo(unsigned u) { return __builtin_bit_cast(float, u << 16); }
__device__ __forceinline__ float bf_hi(unsigned u) { return __builtin_bit_cast(float, u & 0xffff0000u); }
__device__ __forceinline__ __amdgpu_buffer_rsrc_t mkrsrc(const void* p) {
  return __builtin_amdgcn_make_buffer_rsrc((void*)p, 0, 0x7fffffff, 0x00020000);
}
__device__ __forceinline__ void blds16(__amdgpu_buffer_rsrc_t r, unsigned voff, char* l) {
  __builtin_amdgcn_raw_ptr_buffer_load_lds(r, (LDS_AS void*)l, 16, voff, 0, 0, 0);
}
__device__ __forceinline__ bf16x8 tr_read2(const char* p0, const char* p1) {
  s16x4 t0 = __builtin_amdgcn_ds_read_tr16_b64_v4i16((LDS_AS s16x4*)(p0));
  s16x4 t1 = __builtin_amdgcn_ds_read_tr16_b64_v4i16((LDS_AS s16x4*)(p1));
  return __builtin_shufflevector(t0, t1, 0, 1, 2, 3, 4, 5, 6, 7);
}
#define MFMA16(a, b, c) __builtin_amdgcn_mfma_f32_16x16x32_bf16(a, b, c, 0, 0, 0)
__device__ __forceinline__ bf16x8 split3(float c, bool z) {
  const float hi = bf16_round(c), r1 = c - hi, mid = bf16_round(r1), lo = r1 - mid;
  u32x4 v = {pack_bf16(hi, mid), pack_bf16(lo, 0.f), 0u, 0u};
  if (z) v = u32x4{0u, 0u, 0u, 0u};
  return __builtin_bit_cast(bf16x8, v);
}

constexpr int K1_W = 65536, K1_WS = 24576;
constexpr int K1_CODE_LINES = 120, K2_CODE_LINES = 84;
constexpr int KB0 = 4 * NA * 64;
constexpr int NB = (DFF - KB0) / 128;

__device__ __forceinline__ void k1_loader(char* smem, int lw, const u16* __restrict__ x, const char* wb, bool nc, int m0,
                                          const u16* pfa, const u16* pfb, const u16* pfc, const char* code2,
                                          const char* wsb, const char* outb) {
  const int tid = threadIdx.x, lane = tid & 63;
  STAMP_DECL
  RSTAMP(14);
  STAMP(0);
  const int xcd = blockIdx.x & 7, idx = blockIdx.x >> 3;
  unsigned vx[4], vw[6], pfd = 0;
#pragma unroll
  for (int i = 0; i < 4; ++i)
    vx[i] = ((m0 + 32 * lw + 8 * i + (lane >> 3)) * (D_ * 2) + (((lane & 7) ^ ((lane >> 4) & 3)) * 16)) ^ ((i & 1) << 6);
  const int wstep = nc ? 64 * (DFF * 2) : 128;
  auto stage_x = [&](int t) {
    const __amdgpu_buffer_rsrc_t px = mkrsrc((const char*)x + t * 128);
#pragma unroll
    for (int i = 0; i < 4; ++i) blds16(px, vx[i], smem + (t & 3) * 16384 + (4 * lw + i) * 1024);
  };
  auto stage_w = [&](int t) {
    const __amdgpu_buffer_rsrc_t pw = mkrsrc(wb + t * wstep);
#pragma unroll
    for (int i = 0; i < 6; ++i) blds16(pw, vw[i], smem + K1_W + (t & 3) * K1_WS + (6 * lw + i) * 1024);
  };
  stage_x(0);
  asm volatile("" : "+s"(wb));
  __builtin_amdgcn_s_setprio(3);
  if (nc) {
#pragma unroll
    for (int i = 0; i < 3; ++i) {
      const int id = 64 * i + lane, kr = id / 24, c = id % 24;
      vw[i] = (16 * lw + kr) * (DFF * 2) + ((c ^ (((kr >> 1) & 1) << 2)) * 16);
      vw[i + 3] = (vw[i] ^ 32) + 8 * (DFF * 2);
    }
  } else {
#pragma unroll
    for (int i = 0; i < 6; ++i)
      vw[i] = ((48 * lw + 8 * i + (lane >> 3)) * (D_ * 2) + (((lane & 7) ^ ((lane >> 4) & 3)) * 16)) ^ ((i & 1) << 6);
  }
#pragma unroll
  for (int i = 0; i < 6; ++i) asm volatile("" : "+v"(vw[i]));
  stage_w(0);
  stage_x(1);
  stage_w(1);
  SCHED();
  const char* ta[4];
  {
    const int ksl = xcd >> 1, t2 = tid - 256;
    constexpr int LA = 2 * NA * 8, LB = (DFF - KB0) / 32 * 4;
    const int c = (2 * (idx >> 2) + 4 * (t2 & 1) + ((t2 >> 1) & 1)) & 7;
    ta[0] = (const char*)((t2 & 4) ? pfb : pfa) + (1024 * (xcd & 1) + 32 * idx + (t2 >> 3)) * 4096 + 1024 * ksl + c * 128;
    ta[1] = (const char*)((t2 & 4) ? pfb : pfa) + (1024 * (xcd & 1) + 32 * idx + (t2 >> 3)) * 4096 + 1024 * ksl + (c ^ 2) * 128;
    ta[0] = (const char*)((t2 & 128) ? pfb : pfa) + (1024 * (xcd & 1) + 32 * idx + ((t2 >> 3) & 15)) * 4096 + 1024 * ksl + (t2 & 7) * 128;
    ta[1] = ta[0] + 16 * 4096;
    const int tb = min(max(t2 - LA, 0), LB - 1);
    ta[2] = (const char*)pfc + (t2 < LA ? (64 * NA * ksl + 2 * NA * idx + (t2 >> 3)) * 1024 + (t2 & 7) * 128
                                        : (KB0 + (LB / 4) * idx + (tb >> 2)) * 1024 + (tb & 3) * 256 + (ksl & 1) * 128);
    const int j = min(11 * lw + min(lane, 10), 43);
    ta[3] = j < 37 ? wsb + 32768 + j * 524288 : j < 41 ? outb + (j - 37) * 524288
                   : code2 + min(3 * idx + j - 41, K2_CODE_LINES - 1) * 128;
  }
  constexpr int TP[2] = {TSL};
  const int tp0 = ST0, tp1 = ST1;
  auto ntouch = [&](int sg) { return (TP[0] == sg) + (TP[1] == sg); };
  asm volatile("" : "+v"(ta[0]), "+v"(ta[1]), "+v"(ta[2]), "+v"(ta[3]));
  STAMP(1);
  WAIT_V(10);
  BAR();
  STAMP(2);
  stage_x(2);
  stage_w(2);
  if (tp0 == 2) TOUCH64(pfd, ta[0]);
#pragma unroll
  for (int t = 0; t < 8; ++t) {
    if (t < 7) {
      const int n = t + 1 < 7 ? ntouch(t + 1) + 10 + ntouch(t + 2) : ntouch(7);
      const int d = (tp0 == t + 1) + (tp0 == t + 2) + (tp1 == t + 1) + (tp1 == t + 2);
      if (d == 0) wait_v(n);
      else if (d == 1) wait_v(n + 1);
      else wait_v(n + 2);
    }
    BAR();
    if (t + 3 < 8) {
      stage_x(t + 3);
      stage_w(t + 3);
      if (TP[1] == t + 3) TOUCH64(pfd, ta[3]);
      if (tp0 == t + 3) TOUCH64(pfd, ta[0]);
      if (tp1 == t + 3) TOUCH64(pfd, ta[1]);
      if (TP[0] == t + 3) TOUCH64(pfd, ta[2]);
    }
  }
  STAMP(3);
  BAR();
  WAIT_V(0);
  asm volatile("" ::"v"(pfd));
  STAMP(12);
  STAMP_FLUSH(0);
}

template <bool NC>
__device__ __forceinline__ void k1_body(char* smem, int wid, u16* __restrict__ dst, int m0, int col0, bool skip64, float sgn,
                                        const char* code1) {
  const int tid = threadIdx.x, lane = tid & 63;
  STAMP_DECL
  RSTAMP(14);
  STAMP(0);
  unsigned cfd = 0;
  if (CPF1 && (unsigned)(tid - 64) < 5u) TOUCH(cfd, min(5 * (int)(blockIdx.x >> 3) + tid - 64, K1_CODE_LINES - 1) * 128, code1);
  float* stat = (float*)smem;
  const int wr = wid >> 1, wc = wid & 1;
  const int r16 = lane & 15, g = lane >> 4;
  int aoff[2][2], woff[2][4];
#pragma unroll
  for (int s = 0; s < 2; ++s)
#pragma unroll
    for (int e = 0; e < 2; ++e)
      aoff[s][e] = (64 * wr + 16 * (e ^ wc) + r16) * 128 + (((4 * s + g) ^ (r16 >> 1)) * 16);
#pragma unroll
  for (int j = 0; j < 4; ++j) {
    if constexpr (NC) {
      const int q = r16 >> 2, p = lane & 3;
      woff[0][j] = K1_W + (8 * g + q) * 384 + (((6 * wc + (j ^ wr)) ^ (q & 2) ^ (g & 1)) * 32) + p * 8;
    } else {
      woff[0][j] = K1_W + (96 * wc + 16 * ((j & 1) ^ wr) + r16) * 128 + (((4 * (j >> 1) + g) ^ (r16 >> 1)) * 16);
    }
    woff[1][j] = woff[0][j] + 2 * K1_WS;
    asm volatile("" : "+v"(woff[1][j]));
  }
  const f32x4 z4 = {0.f, 0.f, 0.f, 0.f};
  f32x4 acc[4][6], s1[2] = {z4, z4}, s2[2] = {z4, z4}, cs[3] = {z4, z4, z4};
#pragma unroll
  for (int v = 0; v < 6; ++v)
#pragma unroll
    for (int u = 0; u < 4; ++u) acc[u][v] = z4;
  bf16x8 ones;
#pragma unroll
  for (int e = 0; e < 8; ++e) ones[e] = (short)0x3F80;
  bf16x8 xa[2][4];
  s16x4 wf[2][6][2];
  auto rdn = [&](int set, int b, int n) {
    constexpr int TX[16] = {-1, -1, 0, 1, -1, -1, 2, -1, -1, 3, -1, -1, -1, -1, -1, -1};
    constexpr int TW[16] = {0, 1, -1, -1, 2, 3, -1, 4, 5, -1, 6, 7, 8, 9, 10, 11};
    if (TX[n] >= 0) {
      const int u = TX[n];
      xa[set][u] = *(const bf16x8*)(smem + aoff[set][u & 1] + (u >> 1) * 4096 + b * 16384);
    } else if constexpr (NC) {
      const int v = TW[n] >> 1, hi = TW[n] & 1;
      wf[set][v][hi] = __builtin_amdgcn_ds_read_tr16_b64_v4i16(
          (LDS_AS s16x4*)(smem + woff[b >> 1][v & 3] + (b & 1) * K1_WS + set * 12288 + hi * 1536 + (v >> 2) * 128));
    } else if (!(TW[n] & 1)) {
      const int v = TW[n] >> 1;
      const bf16x8 t = *(const bf16x8*)(smem + woff[b >> 1][2 * set + (v & 1)] + (b & 1) * K1_WS + (v >> 1) * 4096);
      wf[set][v][0] = __builtin_shufflevector(t, t, 0, 1, 2, 3);
      wf[set][v][1] = __builtin_shufflevector(t, t, 4, 5, 6, 7);
    }
  };
#define WF(set, v) __builtin_shufflevector(wf[set][v][0], wf[set][v][1], 0, 1, 2, 3, 4, 5, 6, 7)
  auto half = [&](int set, int b) {
    constexpr int OV[24] = {0, 0, 1, 1, 0, 1, 2, 2, 2, 0, 1, 2, 3, 3, 3, 3, 4, 4, 4, 4, 5, 5, 5, 5};
    constexpr int OU[24] = {0, 1, 0, 1, 2, 2, 0, 1, 2, 3, 3, 3, 0, 1, 2, 3, 0, 1, 2, 3, 0, 1, 2, 3};
    WAIT_L(NC ? 6 : 3);
#pragma unroll
    for (int n = 0; n < 24; ++n) {
      acc[OU[n]][OV[n]] = MFMA16(WF(set, OV[n]), xa[set][OU[n]], acc[OU[n]][OV[n]]);
      SCHED();
      if (n < 16) rdn(set ^ 1, b, n);
      if (n == 2 || n == 8) s1[n / 6] = MFMA16(ones, xa[set][2 * (n / 6)], s1[n / 6]);
      if (n == 5 || n == 11) s2[n / 6] = MFMA16(xa[set][2 * (n / 6)], xa[set][2 * (n / 6)], s2[n / 6]);
      if (n == 14 || n == 17 || n == 20) cs[(n - 14) / 3] = MFMA16(ones, WF(set, 2 * ((n - 14) / 3)), cs[(n - 14) / 3]);
      SCHED();
    }
  };
#pragma unroll
  for (int v = 0; v < 6; ++v) asm volatile("" : "+v"(acc[0][v]), "+v"(acc[1][v]), "+v"(acc[2][v]), "+v"(acc[3][v]));
  asm volatile("" : "+v"(s1[0]), "+v"(s1[1]), "+v"(s2[0]), "+v"(s2[1]), "+v"(cs[0]), "+v"(cs[1]), "+v"(cs[2]), "+v"(ones));
  WAIT_L(0);
  BAR();
  STAMP(2);
#pragma unroll
  for (int n = 0; n < 16; ++n) rdn(0, 0, n);
#pragma unroll 1
  for (int it = 0; it < 2; ++it) {
#pragma unroll
    for (int t = 0; t < 4; ++t) {
      SCHED();
      half(0, t);
      BAR();
      SCHED();
      half(1, (t + 1) & 3);
    }
  }
  STAMP(3);
  WAIT_V(0);
  asm volatile("" ::"v"(cfd));
  if (g == 0) {
#pragma unroll
    for (int w = 0; w < 3; ++w) stat[256 + 96 * wc + 16 * (2 * w + wr) + r16] = cs[w][0];
  }
  if (g == (r16 >> 2)) {
#pragma unroll
    for (int rg = 0; rg < 2; ++rg) {
      const float d = (lane & 2) ? ((lane & 1) ? s2[rg][3] : s2[rg][2]) : ((lane & 1) ? s2[rg][1] : s2[rg][0]);
      const float mean = s1[rg][0] * (1.f / 512.f);
      const float var = fmaxf(d * (1.f / 512.f) - mean * mean, 0.f);
      const float den = bf16_round(__builtin_amdgcn_sqrtf(bf16_round(bf16_round(var) + 1e-5f)));
      *(f32x2*)(stat + 2 * (64 * wr + 16 * (2 * rg + wc) + r16)) = f32x2{-mean, sgn * __builtin_amdgcn_rcpf(den)};
    }
  }
  WAIT_L0();
  BAR();
  STAMP(4);
  f32x2 st[4];
  float co[3];
#pragma unroll
  for (int u = 0; u < 4; ++u) st[u] = *(const f32x2*)(stat + 2 * (64 * wr + 16 * (u ^ wc) + r16));
#pragma unroll
  for (int w = 0; w < 3; ++w) co[w] = stat[256 + 96 * wc + 16 * (2 * w + (wr ^ 1)) + r16];
  bf16x8 xm[4];
#pragma unroll
  for (int u = 0; u < 4; ++u) {
    const unsigned nm = pack_bf16(st[u][0], st[u][0]);
    xm[u] = __builtin_bit_cast(bf16x8, u32x4{nm, nm & 0xffffu, 0u, 0u});
  }
  constexpr int NCH = NC ? DFF / 8 : D_ / 8;
  const __amdgpu_buffer_rsrc_t ro = mkrsrc((const char*)dst + ((size_t)(m0 >> 3) * NCH + col0 / 8) * 128);
  unsigned vo[4];
#pragma unroll
  for (int u = 0; u < 4; ++u)
    vo[u] = ((8 * wr + 2 * (u ^ wc) + (r16 >> 3)) * NCH + 12 * wc + 2 * ((g & 1) ^ wr) + (g >> 1)) * 128 + (r16 & 7) * 16;
#pragma unroll
  for (int w = 0; w < 3; ++w) {
    if (skip64 && wc == 0 && w < 2) continue;
#pragma unroll
    for (int s = 0; s < 2; ++s) {
      const bf16x8 wm = split3(s ? co[w] : cs[w][0], g != 0);
#pragma unroll
      for (int u = 0; u < 4; ++u) acc[u][2 * w + s] = MFMA16(wm, xm[u], acc[u][2 * w + s]);
    }
#pragma unroll
    for (int u = 0; u < 4; ++u) {
      unsigned pk[2][2];
#pragma unroll
      for (int s = 0; s < 2; ++s)
#pragma unroll
        for (int e = 0; e < 2; ++e) {
          unsigned t = pack_bf16(acc[u][2 * w + s][2 * e] * st[u][1], acc[u][2 * w + s][2 * e + 1] * st[u][1]);
          if constexpr (NC) t = __builtin_bit_cast(unsigned, __builtin_elementwise_max(__builtin_bit_cast(s16x2, t), s16x2{0, 0}));
          pk[s][e] = t;
        }
      const auto r0 = __builtin_amdgcn_permlane16_swap(pk[0][0], pk[1][0], false, false);
      const auto r1 = __builtin_amdgcn_permlane16_swap(pk[0][1], pk[1][1], false, false);
      __builtin_amdgcn_raw_buffer_store_b128(u32x4{r0[0], r1[0], r0[1], r1[1]}, ro, vo[u] + w * 512, 0, 16);
    }
  }
  STAMP(6);
  STAMP(7);
  STAMP_FLUSH(0);
}

__global__ __launch_bounds__(512, 2) void k1_ln_gemm1(const u16* __restrict__ x, const u16* __restrict__ wdre,
                                                      const u16* __restrict__ wdim, const u16* __restrict__ w1,
                                                      u16* __restrict__ ab, u16* __restrict__ h,
                                                      unsigned* __restrict__ cnt, const u16* __restrict__ wsre,
                                                      const u16* __restrict__ wsim, const u16* __restrict__ w2,
                                                      const u16* outp) {
  __shared__ __attribute__((aligned(1024))) char smem[K1_W + 4 * K1_WS];
  const int bid = blockIdx.x, xcd = bid & 7, idx = bid >> 3;
  const int mt = 8 * (xcd & 1) + (idx >> 2), ntile = 4 * (xcd >> 1) + (idx & 3);
  const int m0 = mt * 128;
  const int wid = __builtin_amdgcn_readfirstlane(threadIdx.x >> 6);
  const char* code1;
  int cdist;
  asm volatile("s_getpc_b64 %0\nk1_code_mark:\n\ts_mov_b32 %1, k2_code_mark-k1_code_mark" : "=s"(code1), "=s"(cdist));
  const bool nc = ntile >= 6;
  const int im = ntile >= 3, t = nc ? ntile - 6 : ntile - 3 * im;
  const int col0 = nc ? ((t * 14) / 5) * 64 : t == 2 ? 320 : t * 192;
  if (wid < 4) {
    if (threadIdx.x == 0) cnt[bid * 16] = 0;
    if (!nc) k1_body<false>(smem, wid, ab + (size_t)im * 2048 * D_, m0, col0, t == 2, im ? -1.f : 1.f, code1);
    else k1_body<true>(smem, wid, h, m0, col0, t == 1 || t == 6, 1.f, code1);
    return;
  }
  k1_loader(smem, wid - 4, x, nc ? (const char*)(w1 + col0) : (const char*)((im ? wdim : wdre) + (size_t)col0 * D_), nc, m0,
            wsre, wsim, w2, code1 + cdist, (const char*)cnt, (const char*)outp);
}

constexpr int K2_NA = 16 + NA, K2_NT = K2_NA + NB, K2_B = 65536;

__global__ __launch_bounds__(512, 2) void k2_gemm2(const u16* __restrict__ wsre, const u16* __restrict__ wsim,
                                                   const u16* __restrict__ h, const u16* __restrict__ ab,
                                                   const u16* __restrict__ w2, const u16* __restrict__ x,
                                                   u16* __restrict__ out, char* __restrict__ part,
                                                   unsigned* __restrict__ cnt) {
  __shared__ __attribute__((aligned(1024))) char smem[2 * K2_B];
  asm volatile("k2_code_mark:");
  const int tid = threadIdx.x, lane = tid & 63;
  const int wid = __builtin_amdgcn_readfirstlane(tid >> 6);
  const int bid = blockIdx.x, xcd = bid & 7, idx = bid >> 3;
  const int ksl = xcd >> 1, mt = 8 * (xcd & 1) + (idx >> 2), nt = idx & 3;
  const int ki = ksl >> 1, kj = ksl & 1;
  const int m0 = mt * 128, n0 = nt * 128;
  const int r16 = lane & 15, g = lane >> 4;
  STAMP_DECL
  RSTAMP(14);
  STAMP(0);
  if (wid < 4) {
  const int wr = wid >> 1, wc = wid & 1;
  const int q4 = r16 >> 2, p4 = lane & 3;
  int aoff[2][2], boff[4][2];
#pragma unroll
  for (int s = 0; s < 2; ++s)
#pragma unroll
    for (int i = 0; i < 2; ++i) aoff[s][i] = (64 * (i ^ ki) + 32 * wr + r16) * 128 + (((4 * s + g) ^ (r16 >> 1)) * 16);
#pragma unroll
  for (int f = 0; f < 4; ++f) {
    boff[f][0] = K2_B + g * 2048 + (8 * ((f >> 1) ^ kj) + 4 * wc + 2 * (f & 1) + (p4 >> 1)) * 128 + (q4 + 4 * (g & 1)) * 16 + 8 * (p4 & 1);
    boff[f][1] = boff[f][0] ^ 64;
  }
  const f32x4 z4 = {0.f, 0.f, 0.f, 0.f};
  f32x4 acc[4][4];
#pragma unroll
  for (int a = 0; a < 4; ++a)
#pragma unroll
    for (int b = 0; b < 4; ++b) acc[a][b] = z4;
  bf16x8 am[2][4];
  s16x4 bm[2][4][2];
  auto rdn = [&](int set, int b, int n) {
    constexpr int TA[12] = {-1, -1, 0, 1, -1, -1, 2, -1, -1, 3, -1, -1};
    constexpr int TB[12] = {0, 1, -1, -1, 2, 3, -1, 4, 5, -1, 6, 7};
    if (TA[n] >= 0) am[set][TA[n]] = *(const bf16x8*)(smem + aoff[set][TA[n] >> 1] + (TA[n] & 1) * 2048 + b * 16384);
    else
      bm[set][TB[n] >> 1][TB[n] & 1] = __builtin_amdgcn_ds_read_tr16_b64_v4i16(
          (LDS_AS s16x4*)(smem + boff[TB[n] >> 1][TB[n] & 1] + set * 8192 + b * 16384));
  };
#define BM(set, f) __builtin_shufflevector(bm[set][f][0], bm[set][f][1], 0, 1, 2, 3, 4, 5, 6, 7)
  auto half = [&](int set, int b) {
    constexpr int OB[16] = {0, 0, 1, 1, 0, 1, 2, 2, 2, 0, 1, 2, 3, 3, 3, 3};
    constexpr int OA[16] = {0, 1, 0, 1, 2, 2, 0, 1, 2, 3, 3, 3, 0, 1, 2, 3};
    WAIT_L(3);
#pragma unroll
    for (int n = 0; n < 16; ++n) {
      acc[OA[n]][OB[n]] = MFMA16(BM(set, OB[n]), am[set][OA[n]], acc[OA[n]][OB[n]]);
      SCHED();
      if (n < 12) rdn(set ^ 1, b, n);
      SCHED();
    }
  };
  auto halfb = [&](int set, int b) {
#pragma unroll
    for (int n = 0; n < 8; ++n) {
      const int i = n >> 2, bb = (n >> 1) & 1, a = n & 1;
      acc[a][bb] = MFMA16(BM(set, 2 * i + bb), am[set][2 * i + a], acc[a][bb]);
      SCHED();
      if (n < 6) { rdn(set ^ 1, b, 2 * n); rdn(set ^ 1, b, 2 * n + 1); }
      SCHED();
    }
  };
#pragma unroll
  for (int a = 0; a < 4; ++a) asm volatile("" : "+v"(acc[a][0]), "+v"(acc[a][1]), "+v"(acc[a][2]), "+v"(acc[a][3]));
  WAIT_L(0);
  BAR();
  STAMP(2);
#pragma unroll
  for (int n = 0; n < 12; ++n) rdn(0, 0, n);
#pragma unroll 1
  for (int it = 0; it < 4; ++it) {
#pragma unroll
    for (int t = 0; t < 4; ++t) {
      SCHED();
      half(0, t);
      BAR();
      if (t == 3 && it == 3) {
#pragma unroll
        for (int s = 0; s < 2; ++s)
#pragma unroll
          for (int i = 0; i < 2; ++i) aoff[s][i] = (8 * (i ^ ki) + 4 * wr + (r16 >> 3)) * 1024 + (4 * s + g) * 128 + (r16 & 7) * 16;
#pragma unroll
        for (int f = 0; f < 4; ++f) {
          boff[f][0] = K2_B + (8 * g + q4) * 256 + (((8 * ((f >> 1) ^ kj) + 4 * wc + 2 * (f & 1) + (p4 >> 1)) ^ (q4 << 2) ^ ((g & 1) << 1)) * 16) + 8 * (p4 & 1);
          boff[f][1] = boff[f][0] + 1024;
        }
      }
      SCHED();
      half(1, (t + 1) & 3);
    }
  }
#pragma unroll
  for (int t = 16; t < K2_NA; ++t) {
    SCHED();
    half(0, t & 3);
    BAR();
    SCHED();
    half(1, (t + 1) & 3);
  }
  STAMP(8);
  const int tile = mt * 4 + nt;
  const int voff = wid * 2048 + lane * 16;
#pragma unroll
  for (int s = 1; s < 4; ++s) {
    const __amdgpu_buffer_rsrc_t rp = mkrsrc(part + ((tile * 4 + (ksl ^ s)) * 3 + (s - 1)) * 8192);
#pragma unroll
    for (int a = 0; a < 2; ++a) {
      const f32x4 c0 = acc[(s & 2) + a][2 * (s & 1)], c1 = acc[(s & 2) + a][2 * (s & 1) + 1];
      const u32x4 v = {pack_bf16(c0[0], c0[1]), pack_bf16(c0[2], c0[3]), pack_bf16(c1[0], c1[1]), pack_bf16(c1[2], c1[3])};
      __builtin_amdgcn_raw_buffer_store_b128(v, rp, voff + a * 1024, 0, 16);
    }
  }
  STAMP(9);
  const __amdgpu_buffer_rsrc_t rq = mkrsrc(part + (tile * 4 + ksl) * 3 * 8192);
  const size_t obase = (size_t)(m0 + 64 * ki + 32 * wr + r16) * D_ + n0 + 64 * kj + 32 * wc + 16 * (g & 1) + 8 * (g >> 1);
  GLB_AS unsigned* mine = (GLB_AS unsigned*)(cnt + (tile * 4 + ksl) * 16);
  u32x4 xv[2], pv[3][2];
  unsigned seen = 0;
#pragma unroll
  for (int t = 0; t < NB; ++t) {
    SCHED();
    halfb(0, (K2_NA + t) & 3);
    if (t == SIG_T) { STAMP(10); WAIT_V(0); STAMP(11); }
    if (t == POLL_T && wid == 0) {
      STAMP(4);
      for (int spin = 0; spin < (1 << 22) && __builtin_amdgcn_readfirstlane(seen) < 3u; ++spin) {
        __builtin_amdgcn_s_sleep(1);
        seen = __hip_atomic_load(mine, __ATOMIC_RELAXED, __HIP_MEMORY_SCOPE_AGENT);
      }
      STAMP(5);
    }
    BAR();
    if (t == SIG_T && wid == 0 && lane >= 1 && lane < 4)
      __hip_atomic_fetch_add((GLB_AS unsigned*)(cnt + (tile * 4 + (ksl ^ lane)) * 16), 1u, __ATOMIC_RELAXED, __HIP_MEMORY_SCOPE_AGENT);
    if (t == XL_T) {
#pragma unroll
      for (int a = 0; a < 2; ++a) xv[a] = *(const u32x4*)(x + obase + (size_t)16 * D_ * a);
    }
    if (t == POLL_T - POLL_D && wid == 0) seen = __hip_atomic_load(mine, __ATOMIC_RELAXED, __HIP_MEMORY_SCOPE_AGENT);
    if (t == POLL_T) {
#pragma unroll
      for (int s = 0; s < 3; ++s)
#pragma unroll
        for (int a = 0; a < 2; ++a)
          pv[s][a] = __builtin_bit_cast(u32x4, __builtin_amdgcn_raw_buffer_load_b128(rq, voff + a * 1024 + s * 8192, 0, 16));
    }
    SCHED();
    halfb(1, (K2_NA + t + 1) & 3);
  }
  STAMP(6);
#pragma unroll
  for (int a = 0; a < 2; ++a) {
    const auto x0 = __builtin_amdgcn_permlane16_swap(xv[a][0], xv[a][2], false, false);
    const auto x1 = __builtin_amdgcn_permlane16_swap(xv[a][1], xv[a][3], false, false);
    const unsigned xx[4] = {x0[0], x1[0], x0[1], x1[1]};
    unsigned o[4];
#pragma unroll
    for (int e = 0; e < 4; ++e) {
      const unsigned p0 = pv[0][a][e], p1 = pv[1][a][e], p2 = pv[2][a][e];
      const float lo = ((acc[a][e >> 1][2 * (e & 1)] + bf_lo(p0)) + (bf_lo(p1) + bf_lo(p2))) + bf_lo(xx[e]);
      const float hi = ((acc[a][e >> 1][2 * (e & 1) + 1] + bf_hi(p0)) + (bf_hi(p1) + bf_hi(p2))) + bf_hi(xx[e]);
      o[e] = pack_bf16(lo, hi);
    }
    const auto r0 = __builtin_amdgcn_permlane16_swap(o[0], o[2], false, false);
    const auto r1 = __builtin_amdgcn_permlane16_swap(o[1], o[3], false, false);
    *(u32x4*)(out + obase + (size_t)16 * D_ * a) = u32x4{r0[0], r1[0], r0[1], r1[1]};
  }
  STAMP(7);
  STAMP(12);
  STAMP_FLUSH(1);
  return;
  }
  {
    __builtin_amdgcn_s_setprio(3);
    const int lw = wid - 4;
    const int l3 = lane >> 3, l7 = lane & 7;
    unsigned va[3][4], vb[3][4];
#pragma unroll
    for (int i = 0; i < 4; ++i) {
      va[0][i] = ((m0 + 32 * lw + 8 * i + l3) * 4096 + ((l7 ^ (g & 3)) * 16)) ^ ((i & 1) << 6);
      vb[0][i] = ((2 * lw + (i >> 1)) * 64 + n0 / 8 + 8 * (i & 1) + l3) * 128 + ((l7 ^ (4 * (i >> 1))) * 16);
    }
    int lbuf = 0;
    auto stage = [&](const unsigned (&oa)[4], const unsigned (&ob)[4], const char* pa, const char* pb) {
      char* lb = smem + lbuf * 16384 + lw * 4096;
      lbuf = (lbuf + 1) & 3;
      const __amdgpu_buffer_rsrc_t ra = mkrsrc(pa), rb = mkrsrc(pb);
#pragma unroll
      for (int i = 0; i < 4; ++i) blds16(rb, ob[i], lb + K2_B + i * 1024);
#pragma unroll
      for (int i = 0; i < 4; ++i) blds16(ra, oa[i], lb + i * 1024);
    };
    auto run = [&](int n, const unsigned (&oa)[4], const unsigned (&ob)[4], const char* pa, const char* pb, int sa, int sb) {
#pragma unroll 1
      for (int t = 0; t < n; ++t, pa += sa, pb += sb) {
        WAIT_V(8);
        BAR();
        stage(oa, ob, pa, pb);
      }
    };
    int ks = ROT2 == 1 ? (4 * nt + 2 * (idx >> 2)) & 15 : ROT2 == 2 ? (8 * ((idx >> 2) & 1)) : ROT2 == 3 ? (4 * ((idx >> 2) & 3)) : 0;
    auto stage0 = [&]() {
      stage(va[0], vb[0], (const char*)((ks & 8) ? wsim : wsre) + 1024 * ksl + 128 * (ks & 7),
            (const char*)ab + ksl * 524288 + (ks >> 3) * 2097152 + (ks & 7) * 65536);
      ks = (ks + 1) & 15;
    };
    stage0();
    stage0();
    STAMP(1);
    WAIT_V(8);
    BAR();
    stage0();
    SCHED();
#pragma unroll
    for (int i = 0; i < 4; ++i) {
      const int c = r16 ^ (g << 2) ^ ((i >> 1) << 1);
      va[1][i] = ((m0 / 8 + 4 * lw + i) * (DFF / 8) + l3) * 128 + l7 * 16;
      vb[1][i] = (16 * lw + 4 * i + g) * 1024 + n0 * 2 + c * 16;
      va[2][i] = ((m0 / 8 + 8 * ki + 4 * (lw & 1) + i) * (DFF / 8) + 8 * (lw >> 1) + l3) * 128 + l7 * 16;
      vb[2][i] = (64 * ((c >> 3) ^ kj ^ ki) + 16 * lw + 4 * i + g) * 1024 + (n0 + 64 * kj) * 2 + (c & 7) * 16;
    }
#pragma unroll
    for (int i = 0; i < 4; ++i) asm volatile("" : "+v"(va[1][i]), "+v"(vb[1][i]), "+v"(va[2][i]), "+v"(vb[2][i]));
#pragma unroll 1
    for (int t = 0; t < 13; ++t) {
      WAIT_V(8);
      BAR();
      stage0();
    }
    STAMP(4);
    run(NA, va[1], vb[1], (const char*)h + 1024 * NA * ksl, (const char*)w2 + (size_t)65536 * NA * ksl, 1024, 65536);
    STAMP(5);
    {
      const int kb = ROT3 ? (2 * nt + (idx >> 2)) % NB : 0;
      run(NB - kb, va[2], vb[2], (const char*)h + KB0 * 16 + kb * 2048, (const char*)w2 + (size_t)KB0 * 1024 + kb * 131072, 2048, 131072);
      run(kb, va[2], vb[2], (const char*)h + KB0 * 16, (const char*)w2 + (size_t)KB0 * 1024, 2048, 131072);
    }
    STAMP(6);
    WAIT_V(8);
    BAR();
    WAIT_V(0);
    BAR();
    BAR();
    STAMP(8);
    STAMP_FLUSH(1);
  }
}

extern "C" void kernel_launch(void* const* d_in, const int* in_sizes, int n_in, void* d_out, int out_size,
                              void* d_ws, size_t ws_size, hipStream_t stream) {
  const u16* x = (const u16*)d_in[0];
  const u16* wsre = (const u16*)d_in[1];
  const u16* wsim = (const u16*)d_in[2];
  const u16* wdre = (const u16*)d_in[3];
  const u16* wdim = (const u16*)d_in[4];
  const u16* w1 = (const u16*)d_in[6];
  const u16* w2 = (const u16*)d_in[7];
  char* ws = (char*)d_ws;
  unsigned* cnt = (unsigned*)(ws + WS_CNT);
  u16* ab = (u16*)(ws + WS_AB);
  u16* h = (u16*)(ws + WS_H);
  k1_ln_gemm1<<<256, 512, 0, stream>>>(x, wdre, wdim, w1, ab, h, cnt, wsre, wsim, w2, (const u16*)d_out);
  k2_gemm2<<<256, 512, 0, stream>>>(wsre, wsim, h, ab, w2, x, (u16*)d_out, ws + WS_P, cnt);
}
```

```cpp
#include <hip/hip_runtime.h>
#include <hip/hip_bf16.h>
#include <stdint.h>

typedef __attribute__((ext_vector_type(8))) short bf16x8;
typedef __attribute__((ext_vector_type(4))) short s16x4;
typedef __attribute__((ext_vector_type(2))) short s16x2;
typedef __attribute__((ext_vector_type(4))) float f32x4;
typedef __attribute__((ext_vector_type(2))) float f32x2;
typedef __attribute__((ext_vector_type(2))) __bf16 bf16x2_t;
typedef __attribute__((ext_vector_type(4))) unsigned u32x4;
typedef __attribute__((ext_vector_type(2))) unsigned u32x2;
typedef __attribute__((ext_vector_type(4))) unsigned long long u64x4;
typedef unsigned short u16;

#define LDS_AS __attribute__((address_space(3)))
#define GLB_AS __attribute__((address_space(1)))
#define WAIT_V(n) asm volatile("s_waitcnt vmcnt(%0)" ::"n"(n) : "memory")
#define WAIT_L0() asm volatile("s_waitcnt lgkmcnt(0)" ::: "memory")
#define WAIT_L(n) __builtin_amdgcn_s_waitcnt(0xC07F | ((n) << 8))
#define BAR() do { asm volatile("" ::: "memory"); __builtin_amdgcn_s_barrier(); asm volatile("" ::: "memory"); } while (0)
#define SCHED() __builtin_amdgcn_sched_barrier(0)

#define STAMP_DECL
#define STAMP(i)
#define RSTAMP(i)
#define STAMP_FLUSH(k)

#define BLK_LAYOUT 1
constexpr int S_ = 2048, D_ = 512, DFF = 1792;
constexpr int HLD = DFF;

constexpr size_t WS_CNT = 0;
constexpr size_t WS_AB = 65536;
constexpr size_t WS_H = WS_AB + (size_t)4096 * 512 * 2;
constexpr size_t WS_P = WS_H + (size_t)2048 * 2048 * 2;

#define TSL 7, 6
#define NA 5
#define SIG_T 1
#define POLL_T 3
#define POLL_D 1
#define XL_T (POLL_T - 2)
#define ROT1 0
#define ROT2 2
#define ROT3 1
#define CPF1 1
#define CPF2 1
#define TPAT 2
#define ST0 (2 + (idx % 5))
#define ST1 7
#define K1CL 160
#define EARLY 1
#define EARLY2 1
#define EARLY_N 16
#define BALC 1
#define PGT 1
#define TPOL ""
#define TOUCH64(d, p) asm volatile("global_load_dword %0, %1, off" TPOL : "+v"(d) : "v"(p) : "memory")
#define TOUCH(d, off, base) asm volatile("s_nop 4\n\tglobal_load_dword %0, %1, %2" : "+v"(d) : "v"(off), "s"(base) : "memory")

__device__ __forceinline__ void wait_v(int n) {
  switch (n) {
#define WV(k) case k: WAIT_V(k); break;
    WV(0) WV(1) WV(2) WV(3) WV(4) WV(5) WV(6) WV(10) WV(11) WV(12) WV(13) WV(14) WV(15) WV(16)
#undef WV
    default: WAIT_V(0);
  }
}
__device__ __forceinline__ float bf16_round(float f) { return __bfloat162float(__float2bfloat16(f)); }
__device__ __forceinline__ unsigned pack_bf16(float lo, float hi) {
  f32x2 v = {lo, hi};
  return __builtin_bit_cast(unsigned, __builtin_convertvector(v, bf16x2_t));
}
__device__ __forceinline__ float bf_lo(unsigned u) { return __builtin_bit_cast(float, u << 16); }
__device__ __forceinline__ float bf_hi(unsigned u) { return __builtin_bit_cast(float, u & 0xffff0000u); }
__device__ __forceinline__ __amdgpu_buffer_rsrc_t mkrsrc(const void* p) {
  return __builtin_amdgcn_make_buffer_rsrc((void*)p, 0, 0x7fffffff, 0x00020000);
}
__device__ __forceinline__ void blds16(__amdgpu_buffer_rsrc_t r, unsigned voff, char* l) {
  __builtin_amdgcn_raw_ptr_buffer_load_lds(r, (LDS_AS void*)l, 16, voff, 0, 0, 0);
}
__device__ __forceinline__ bf16x8 tr_read2(const char* p0, const char* p1) {
  s16x4 t0 = __builtin_amdgcn_ds_read_tr16_b64_v4i16((LDS_AS s16x4*)(p0));
  s16x4 t1 = __builtin_amdgcn_ds_read_tr16_b64_v4i16((LDS_AS s16x4*)(p1));
  return __builtin_shufflevector(t0, t1, 0, 1, 2, 3, 4, 5, 6, 7);
}
#define MFMA16(a, b, c) __builtin_amdgcn_mfma_f32_16x16x32_bf16(a, b, c, 0, 0, 0)
__device__ __forceinline__ bf16x8 split3(float c, bool z) {
  const float hi = bf16_round(c), r1 = c - hi, mid = bf16_round(r1), lo = r1 - mid;
  u32x4 v = {pack_bf16(hi, mid), pack_bf16(lo, 0.f), 0u, 0u};
  if (z) v = u32x4{0u, 0u, 0u, 0u};
  return __builtin_bit_cast(bf16x8, v);
}

constexpr int K1_W = 65536, K1_WS = 24576;
constexpr int K1_CODE_LINES = K1CL, K2_CODE_LINES = 84;
constexpr int KB0 = 4 * NA * 64;
constexpr int NB = (DFF - KB0) / 128;

struct Tile { bool nc, im, skip; int col0; };
__device__ __forceinline__ Tile tile_of(int bid) {
  const int ntile = 4 * ((bid & 7) >> 1) + ((bid >> 3) & 3);
  const unsigned long long G = BALC ? 0ull | 3ull << 6 | 37ull << 12 | 0ull << 18 | 3ull << 24 | 37ull << 30 | 0ull << 36 | 5ull << 42 | 34ull << 48 | 8ull << 54
                                    : 0ull | 3ull << 6 | 37ull << 12 | 0ull << 18 | 3ull << 24 | 37ull << 30 | 0ull << 36 | 34ull << 42 | 5ull << 48 | 8ull << 54;
  const unsigned long long H = 11ull | 14ull << 6 | 48ull << 12 | 19ull << 18 | 22ull << 24 | 25ull << 30;
  const unsigned e = (unsigned)((ntile < 10 ? G : H) >> (6 * (ntile < 10 ? ntile : ntile - 10)));
  return Tile{ntile >= 6, ntile >= 3, (e & 32) != 0, (int)(e & 31) * 64};
}

__device__ __forceinline__ void k1_loader(char* smem, int lw, u64x4 ka, int m0,
                                          const u16* pfa, const u16* pfb, const u16* pfc, const char* code2,
                                          const char* wsb, const char* outb, unsigned kd1, unsigned pfd, unsigned cf1) {
  const int tid = threadIdx.x, lane = tid & 63;
  STAMP_DECL
  RSTAMP(14);
  STAMP(0);
  const int xcd = blockIdx.x & 7, idx = blockIdx.x >> 3;
  unsigned vx[4], vw[6];
#pragma unroll
  for (int i = 0; i < 4; ++i)
    vx[i] = ((m0 + 32 * lw + 8 * i + (lane >> 3)) * (D_ * 2) + (((lane & 7) ^ ((lane >> 4) & 3)) * 16)) ^ ((i & 1) << 6);
  const char *x, *kp1, *kp2, *kp3;
  asm volatile("s_waitcnt lgkmcnt(0)\n\ts_mov_b64 %0, %4\n\ts_mov_b64 %1, %5\n\ts_mov_b64 %2, %6\n\ts_mov_b64 %3, %7"
               : "=&s"(x), "=&s"(kp1), "=&s"(kp2), "=&s"(kp3) : "s"(ka[0]), "s"(ka[1]), "s"(ka[2]), "s"(ka[3]), "s"(kd1));
  const char* wb;
  int wstep;
  auto stage_x = [&](int t) {
    const __amdgpu_buffer_rsrc_t px = mkrsrc((const char*)x + t * 128);
#pragma unroll
    for (int i = 0; i < 4; ++i) blds16(px, vx[i], smem + (t & 3) * 16384 + (4 * lw + i) * 1024);
  };
  auto stage_w = [&](int t) {
    const __amdgpu_buffer_rsrc_t pw = mkrsrc(wb + t * wstep);
#pragma unroll
    for (int i = 0; i < 6; ++i) blds16(pw, vw[i], smem + K1_W + (t & 3) * K1_WS + (6 * lw + i) * 1024);
  };
  stage_x(0);
  SCHED();
  int b2 = blockIdx.x;
  asm volatile("" : "+s"(b2));
  const Tile tl = tile_of(b2);
  const bool nc = tl.nc;
  wb = nc ? kp3 + tl.col0 * 2 : (tl.im ? kp2 : kp1) + tl.col0 * (D_ * 2);
  wstep = nc ? 64 * (DFF * 2) : 128;
  __builtin_amdgcn_s_setprio(3);
  if (nc) {
#pragma unroll
    for (int i = 0; i < 3; ++i) {
      const int id = 64 * i + lane, kr = id / 24, c = id % 24;
      vw[i] = (16 * lw + kr) * (DFF * 2) + ((c ^ (((kr >> 1) & 1) << 2)) * 16);
      vw[i + 3] = (vw[i] ^ 32) + 8 * (DFF * 2);
    }
  } else {
#pragma unroll
    for (int i = 0; i < 6; ++i)
      vw[i] = ((48 * lw + 8 * i + (lane >> 3)) * (D_ * 2) + (((lane & 7) ^ ((lane >> 4) & 3)) * 16)) ^ ((i & 1) << 6);
  }
#pragma unroll
  for (int i = 0; i < 6; ++i) asm volatile("" : "+v"(vw[i]));
  stage_w(0);
  stage_x(1);
  stage_w(1);
  SCHED();
  const char* ta[4];
  {
    const int ksl = xcd >> 1, t2 = tid - 256;
    constexpr int LA = 2 * NA * 8, LB = (DFF - KB0) / 32 * 4;
    const int c = (2 * (idx >> 2) + 4 * (t2 & 1) + ((t2 >> 1) & 1)) & 7;
    ta[0] = (const char*)((t2 & 4) ? pfb : pfa) + (1024 * (xcd & 1) + 32 * idx + (t2 >> 3)) * 4096 + 1024 * ksl + c * 128;
    ta[1] = (const char*)((t2 & 4) ? pfb : pfa) + (1024 * (xcd & 1) + 32 * idx + (t2 >> 3)) * 4096 + 1024 * ksl + (c ^ 2) * 128;
    ta[0] = (const char*)((t2 & 128) ? pfb : pfa) + (1024 * (xcd & 1) + 32 * idx + ((t2 >> 3) & 15)) * 4096 + 1024 * ksl + (t2 & 7) * 128;
    ta[1] = ta[0] + 16 * 4096;
    const int tb = min(max(t2 - LA, 0), LB - 1);
    ta[2] = (const char*)pfc + (t2 < LA ? (64 * NA * ksl + 2 * NA * idx + (t2 >> 3)) * 1024 + (t2 & 7) * 128
                                        : (KB0 + (LB / 4) * idx + (tb >> 2)) * 1024 + (tb & 3) * 256 + (ksl & 1) * 128);
    const int j = min(11 * lw + min(lane, 10), 43);
    ta[3] = j < 37 ? wsb + 32768 + j * 524288 : j < 41 ? outb + (j - 37) * 524288
                   : code2 + min(3 * idx + j - 41, K2_CODE_LINES - 1) * 128;
  }
  constexpr int TP[2] = {TSL};
  const int tp0 = ST0, tp1 = ST1;
  auto ntouch = [&](int sg) { return (TP[0] == sg) + (TP[1] == sg); };
  asm volatile("" : "+v"(ta[0]), "+v"(ta[1]), "+v"(ta[2]), "+v"(ta[3]));
  STAMP(1);
  WAIT_V(10);
  asm volatile("" ::"v"(cf1));
  BAR();
  STAMP(2);
  stage_x(2);
  stage_w(2);
  if (tp0 == 2) TOUCH64(pfd, ta[0]);
#pragma unroll
  for (int t = 0; t < 8; ++t) {
    if (t < 7) {
      const int n = t + 1 < 7 ? ntouch(t + 1) + 10 + ntouch(t + 2) : ntouch(7);
      const int d = (tp0 == t + 1) + (tp0 == t + 2) + (tp1 == t + 1) + (tp1 == t + 2);
      if (d == 0) wait_v(n);
      else if (d == 1) wait_v(n + 1);
      else wait_v(n + 2);
    }
    BAR();
    if (t + 3 < 8) {
      stage_x(t + 3);
      stage_w(t + 3);
      if (TP[1] == t + 3) TOUCH64(pfd, ta[3]);
      if (tp0 == t + 3) TOUCH64(pfd, ta[0]);
      if (tp1 == t + 3) TOUCH64(pfd, ta[1]);
      if (TP[0] == t + 3) TOUCH64(pfd, ta[2]);
    }
  }
  STAMP(3);
  BAR();
  WAIT_V(0);
  asm volatile("" ::"v"(pfd));
  STAMP(12);
  STAMP_FLUSH(0);
}

template <bool NC>
__device__ __forceinline__ void k1_body(char* smem, int wid, u16* __restrict__ dst, int m0, int col0, bool skip64, float sgn,
                                        u64x4 ka, unsigned kd1, unsigned cf0, unsigned cf1) {
  const int tid = threadIdx.x, lane = tid & 63;
  STAMP_DECL
  RSTAMP(14);
  STAMP(0);
  float* stat = (float*)smem;
  const int wr = wid >> 1, wc = wid & 1;
  const int r16 = lane & 15, g = lane >> 4;
  int aoff[2][2], woff[2][4];
#pragma unroll
  for (int s = 0; s < 2; ++s)
#pragma unroll
    for (int e = 0; e < 2; ++e)
      aoff[s][e] = (64 * wr + 16 * (e ^ wc) + r16) * 128 + (((4 * s + g) ^ (r16 >> 1)) * 16);
#pragma unroll
  for (int j = 0; j < 4; ++j) {
    if constexpr (NC) {
      const int q = r16 >> 2, p = lane & 3;
      woff[0][j] = K1_W + (8 * g + q) * 384 + (((6 * wc + (j ^ wr)) ^ (q & 2) ^ (g & 1)) * 32) + p * 8;
    } else {
      woff[0][j] = K1_W + (96 * wc + 16 * ((j & 1) ^ wr) + r16) * 128 + (((4 * (j >> 1) + g) ^ (r16 >> 1)) * 16);
    }
    woff[1][j] = woff[0][j] + 2 * K1_WS;
    asm volatile("" : "+v"(woff[1][j]));
  }
  const f32x4 z4 = {0.f, 0.f, 0.f, 0.f};
  f32x4 acc[4][6], s1[2] = {z4, z4}, s2[2] = {z4, z4}, cs[3] = {z4, z4, z4};
#pragma unroll
  for (int v = 0; v < 6; ++v)
#pragma unroll
    for (int u = 0; u < 4; ++u) acc[u][v] = z4;
  bf16x8 ones;
#pragma unroll
  for (int e = 0; e < 8; ++e) ones[e] = (short)0x3F80;
  bf16x8 xa[2][4];
  s16x4 wf[2][6][2];
  auto rdn = [&](int set, int b, int n) {
    constexpr int TX[16] = {-1, -1, 0, 1, -1, -1, 2, -1, -1, 3, -1, -1, -1, -1, -1, -1};
    constexpr int TW[16] = {0, 1, -1, -1, 2, 3, -1, 4, 5, -1, 6, 7, 8, 9, 10, 11};
    if (TX[n] >= 0) {
      const int u = TX[n];
      xa[set][u] = *(const bf16x8*)(smem + aoff[set][u & 1] + (u >> 1) * 4096 + b * 16384);
    } else if constexpr (NC) {
      const int v = TW[n] >> 1, hi = TW[n] & 1;
      wf[set][v][hi] = __builtin_amdgcn_ds_read_tr16_b64_v4i16(
          (LDS_AS s16x4*)(smem + woff[b >> 1][v & 3] + (b & 1) * K1_WS + set * 12288 + hi * 1536 + (v >> 2) * 128));
    } else if (!(TW[n] & 1)) {
      const int v = TW[n] >> 1;
      const bf16x8 t = *(const bf16x8*)(smem + woff[b >> 1][2 * set + (v & 1)] + (b & 1) * K1_WS + (v >> 1) * 4096);
      wf[set][v][0] = __builtin_shufflevector(t, t, 0, 1, 2, 3);
      wf[set][v][1] = __builtin_shufflevector(t, t, 4, 5, 6, 7);
    }
  };
#define WF(set, v) __builtin_shufflevector(wf[set][v][0], wf[set][v][1], 0, 1, 2, 3, 4, 5, 6, 7)
  auto half = [&](int set, int b) {
    constexpr int OV[24] = {0, 0, 1, 1, 0, 1, 2, 2, 2, 0, 1, 2, 3, 3, 3, 3, 4, 4, 4, 4, 5, 5, 5, 5};
    constexpr int OU[24] = {0, 1, 0, 1, 2, 2, 0, 1, 2, 3, 3, 3, 0, 1, 2, 3, 0, 1, 2, 3, 0, 1, 2, 3};
    WAIT_L(NC ? 6 : 3);
#pragma unroll
    for (int n = 0; n < 24; ++n) {
      acc[OU[n]][OV[n]] = MFMA16(WF(set, OV[n]), xa[set][OU[n]], acc[OU[n]][OV[n]]);
      SCHED();
      if (n < 16) rdn(set ^ 1, b, n);
      if (n == 2 || n == 8) s1[n / 6] = MFMA16(ones, xa[set][2 * (n / 6)], s1[n / 6]);
      if (n == 5 || n == 11) s2[n / 6] = MFMA16(xa[set][2 * (n / 6)], xa[set][2 * (n / 6)], s2[n / 6]);
      if (n == 14 || n == 17 || n == 20) cs[(n - 14) / 3] = MFMA16(ones, WF(set, 2 * ((n - 14) / 3)), cs[(n - 14) / 3]);
      SCHED();
    }
  };
#pragma unroll
  for (int v = 0; v < 6; ++v) asm volatile("" : "+v"(acc[0][v]), "+v"(acc[1][v]), "+v"(acc[2][v]), "+v"(acc[3][v]));
  asm volatile("" : "+v"(s1[0]), "+v"(s1[1]), "+v"(s2[0]), "+v"(s2[1]), "+v"(cs[0]), "+v"(cs[1]), "+v"(cs[2]), "+v"(ones));
  asm volatile("s_waitcnt vmcnt(0) lgkmcnt(0)" ::"s"(ka), "s"(kd1), "v"(cf0), "v"(cf1));
  BAR();
  STAMP(2);
#pragma unroll
  for (int n = 0; n < 16; ++n) rdn(0, 0, n);
#pragma unroll 1
  for (int it = 0; it < 2; ++it) {
#pragma unroll
    for (int t = 0; t < 4; ++t) {
      SCHED();
      half(0, t);
      BAR();
      SCHED();
      half(1, (t + 1) & 3);
    }
  }
  STAMP(3);
  if (g == 0) {
#pragma unroll
    for (int w = 0; w < 3; ++w) stat[256 + 96 * wc + 16 * (2 * w + wr) + r16] = cs[w][0];
  }
  if (g == (r16 >> 2)) {
#pragma unroll
    for (int rg = 0; rg < 2; ++rg) {
      const float d = (lane & 2) ? ((lane & 1) ? s2[rg][3] : s2[rg][2]) : ((lane & 1) ? s2[rg][1] : s2[rg][0]);
      const float mean = s1[rg][0] * (1.f / 512.f);
      const float var = fmaxf(d * (1.f / 512.f) - mean * mean, 0.f);
      const float den = bf16_round(__builtin_amdgcn_sqrtf(bf16_round(bf16_round(var) + 1e-5f)));
      *(f32x2*)(stat + 2 * (64 * wr + 16 * (2 * rg + wc) + r16)) = f32x2{-mean, sgn * __builtin_amdgcn_rcpf(den)};
    }
  }
  WAIT_L0();
  BAR();
  STAMP(4);
  f32x2 st[4];
  float co[3];
#pragma unroll
  for (int u = 0; u < 4; ++u) st[u] = *(const f32x2*)(stat + 2 * (64 * wr + 16 * (u ^ wc) + r16));
#pragma unroll
  for (int w = 0; w < 3; ++w) co[w] = stat[256 + 96 * wc + 16 * (2 * w + (wr ^ 1)) + r16];
  bf16x8 xm[4];
#pragma unroll
  for (int u = 0; u < 4; ++u) {
    const unsigned nm = pack_bf16(st[u][0], st[u][0]);
    xm[u] = __builtin_bit_cast(bf16x8, u32x4{nm, nm & 0xffffu, 0u, 0u});
  }
  constexpr int NCH = NC ? DFF / 8 : D_ / 8;
  const __amdgpu_buffer_rsrc_t ro = mkrsrc((const char*)dst + ((size_t)(m0 >> 3) * NCH + col0 / 8) * 128);
  unsigned vo[4];
#pragma unroll
  for (int u = 0; u < 4; ++u)
    vo[u] = ((8 * wr + 2 * (u ^ wc) + (r16 >> 3)) * NCH + 12 * wc + 2 * ((g & 1) ^ wr) + (g >> 1)) * 128 + (r16 & 7) * 16;
#pragma unroll
  for (int w = 0; w < 3; ++w) {
    if (skip64 && wc == 0 && w < 2) continue;
#pragma unroll
    for (int s = 0; s < 2; ++s) {
      const bf16x8 wm = split3(s ? co[w] : cs[w][0], g != 0);
#pragma unroll
      for (int u = 0; u < 4; ++u) acc[u][2 * w + s] = MFMA16(wm, xm[u], acc[u][2 * w + s]);
    }
#pragma unroll
    for (int u = 0; u < 4; ++u) {
      unsigned pk[2][2];
#pragma unroll
      for (int s = 0; s < 2; ++s)
#pragma unroll
        for (int e = 0; e < 2; ++e) {
          unsigned t = pack_bf16(acc[u][2 * w + s][2 * e] * st[u][1], acc[u][2 * w + s][2 * e + 1] * st[u][1]);
          if constexpr (NC) t = __builtin_bit_cast(unsigned, __builtin_elementwise_max(__builtin_bit_cast(s16x2, t), s16x2{0, 0}));
          pk[s][e] = t;
        }
      const auto r0 = __builtin_amdgcn_permlane16_swap(pk[0][0], pk[1][0], false, false);
      const auto r1 = __builtin_amdgcn_permlane16_swap(pk[0][1], pk[1][1], false, false);
      __builtin_amdgcn_raw_buffer_store_b128(u32x4{r0[0], r1[0], r0[1], r1[1]}, ro, vo[u] + w * 512, 0, 16);
    }
  }
  STAMP(6);
  STAMP(7);
  STAMP_FLUSH(0);
}

__global__ __launch_bounds__(512, 2) void k1_ln_gemm1(const u16* __restrict__ x, const u16* __restrict__ wdre,
                                                      const u16* __restrict__ wdim, const u16* __restrict__ w1,
                                                      u16* __restrict__ ab, u16* __restrict__ h,
                                                      unsigned* __restrict__ cnt, const u16* __restrict__ wsre,
                                                      const u16* __restrict__ wsim, const u16* __restrict__ w2,
                                                      const u16* outp) {
  __shared__ __attribute__((aligned(1024))) char smem[K1_W + 4 * K1_WS];
  const char* code1;
  int cdist;
  u64x4 ka;
  unsigned kd1, cf0, cf1;
  asm volatile("s_getpc_b64 %0\nk1_code_mark:\n\ts_load_dwordx8 %2, %4, 0x0\n\ts_load_dword %3, %4, 0x40\n\ts_mov_b32 %1, k2_code_mark-k1_code_mark"
               : "=&s"(code1), "=&s"(cdist), "=&s"(ka), "=&s"(kd1) : "s"(__builtin_amdgcn_kernarg_segment_ptr()));
  asm volatile("v_and_b32 %0, 15, %2\n\tv_lshlrev_b32 %0, 7, %0\n\tglobal_load_dword %0, %0, %3\n\t"
               "v_and_b32 %1, 7, %2\n\tv_add_lshl_u32 %1, %1, %4, 7\n\tglobal_load_dword %1, %1, %3 offset:2048"
               : "=&v"(cf0), "=&v"(cf1) : "v"(threadIdx.x), "s"(code1), "s"(blockIdx.x & 0x78) : "memory");
  SCHED();
  const int wid = __builtin_amdgcn_readfirstlane(threadIdx.x >> 6);
  const int m0 = (8 * (blockIdx.x & 1) + (blockIdx.x >> 5)) * 128;
  if (wid < 4) {
    const Tile tl = tile_of(blockIdx.x);
    if (threadIdx.x == 0) cnt[blockIdx.x * 16] = 0;
    if (!tl.nc) k1_body<false>(smem, wid, ab + (size_t)tl.im * 2048 * D_, m0, tl.col0, tl.skip, tl.im ? -1.f : 1.f, ka, kd1, cf0, cf1);
    else k1_body<true>(smem, wid, h, m0, tl.col0, tl.skip, 1.f, ka, kd1, cf0, cf1);
    return;
  }
  k1_loader(smem, wid - 4, ka, m0, wsre, wsim, w2, code1 + cdist, (const char*)cnt, (const char*)outp, kd1, cf0, cf1);
}

constexpr int K2_NA = 16 + NA, K2_NT = K2_NA + NB, K2_B = 65536;

__global__ __launch_bounds__(512, 2) void k2_gemm2(const u16* __restrict__ wsre, const u16* __restrict__ wsim,
                                                   const u16* __restrict__ h, const u16* __restrict__ ab,
                                                   const u16* __restrict__ w2, const u16* __restrict__ x,
                                                   u16* __restrict__ out, char* __restrict__ part,
                                                   unsigned* __restrict__ cnt) {
  __shared__ __attribute__((aligned(1024))) char smem[2 * K2_B];
  unsigned kd0 = 0, kd1 = 0;
  asm volatile("k2_code_mark:\n\ts_load_dword %0, %2, 0x0\n\ts_load_dword %1, %2, 0x40" : "=&s"(kd0), "=&s"(kd1) : "s"(__builtin_amdgcn_kernarg_segment_ptr()));
  SCHED();
  const int tid = threadIdx.x, lane = tid & 63;
  const int wid = __builtin_amdgcn_readfirstlane(tid >> 6);
  const int bid = blockIdx.x, xcd = bid & 7, idx = bid >> 3;
  const int ksl = xcd >> 1, mt = 8 * (xcd & 1) + (idx >> 2), nt = idx & 3;
  const int ki = ksl >> 1, kj = ksl & 1;
  const int m0 = mt * 128, n0 = nt * 128;
  const int r16 = lane & 15, g = lane >> 4;
  STAMP_DECL
  RSTAMP(14);
  STAMP(0);
  if (wid < 4) {
  const int wr = wid >> 1, wc = wid & 1;
  const int q4 = r16 >> 2, p4 = lane & 3;
  int aoff[2][2], boff[4][2];
#pragma unroll
  for (int s = 0; s < 2; ++s)
#pragma unroll
    for (int i = 0; i < 2; ++i) aoff[s][i] = (64 * (i ^ ki) + 32 * wr + r16) * 128 + (((4 * s + g) ^ (r16 >> 1)) * 16);
#pragma unroll
  for (int f = 0; f < 4; ++f) {
    boff[f][0] = K2_B + g * 2048 + (8 * ((f >> 1) ^ kj) + 4 * wc + 2 * (f & 1) + (p4 >> 1)) * 128 + (q4 + 4 * (g & 1)) * 16 + 8 * (p4 & 1);
    boff[f][1] = boff[f][0] ^ 64;
  }
  const f32x4 z4 = {0.f, 0.f, 0.f, 0.f};
  f32x4 acc[4][4];
#pragma unroll
  for (int a = 0; a < 4; ++a)
#pragma unroll
    for (int b = 0; b < 4; ++b) acc[a][b] = z4;
  bf16x8 am[2][4];
  s16x4 bm[2][4][2];
  auto rdn = [&](int set, int b, int n) {
    constexpr int TA[12] = {-1, -1, 0, 1, -1, -1, 2, -1, -1, 3, -1, -1};
    constexpr int TB[12] = {0, 1, -1, -1, 2, 3, -1, 4, 5, -1, 6, 7};
    if (TA[n] >= 0) am[set][TA[n]] = *(const bf16x8*)(smem + aoff[set][TA[n] >> 1] + (TA[n] & 1) * 2048 + b * 16384);
    else
      bm[set][TB[n] >> 1][TB[n] & 1] = __builtin_amdgcn_ds_read_tr16_b64_v4i16(
          (LDS_AS s16x4*)(smem + boff[TB[n] >> 1][TB[n] & 1] + set * 8192 + b * 16384));
  };
#define BM(set, f) __builtin_shufflevector(bm[set][f][0], bm[set][f][1], 0, 1, 2, 3, 4, 5, 6, 7)
  auto half = [&](int set, int b) {
    constexpr int OB[16] = {0, 0, 1, 1, 0, 1, 2, 2, 2, 0, 1, 2, 3, 3, 3, 3};
    constexpr int OA[16] = {0, 1, 0, 1, 2, 2, 0, 1, 2, 3, 3, 3, 0, 1, 2, 3};
    WAIT_L(3);
#pragma unroll
    for (int n = 0; n < 16; ++n) {
      acc[OA[n]][OB[n]] = MFMA16(BM(set, OB[n]), am[set][OA[n]], acc[OA[n]][OB[n]]);
      SCHED();
      if (n < 12) rdn(set ^ 1, b, n);
      SCHED();
    }
  };
  auto halfb = [&](int set, int b) {
#pragma unroll
    for (int n = 0; n < 8; ++n) {
      const int i = n >> 2, bb = (n >> 1) & 1, a = n & 1;
      acc[a][bb] = MFMA16(BM(set, 2 * i + bb), am[set][2 * i + a], acc[a][bb]);
      SCHED();
      if (n < 6) { rdn(set ^ 1, b, 2 * n); rdn(set ^ 1, b, 2 * n + 1); }
      SCHED();
    }
  };
#pragma unroll
  for (int a = 0; a < 4; ++a) asm volatile("" : "+v"(acc[a][0]), "+v"(acc[a][1]), "+v"(acc[a][2]), "+v"(acc[a][3]));
  asm volatile("s_waitcnt lgkmcnt(0)" ::"s"(kd0), "s"(kd1));
  BAR();
  STAMP(2);
#pragma unroll
  for (int n = 0; n < 12; ++n) rdn(0, 0, n);
#pragma unroll 1
  for (int it = 0; it < 4; ++it) {
#pragma unroll
    for (int t = 0; t < 4; ++t) {
      SCHED();
      half(0, t);
      BAR();
      if (t == 3 && it == 3) {
#pragma unroll
        for (int s = 0; s < 2; ++s)
#pragma unroll
          for (int i = 0; i < 2; ++i) aoff[s][i] = (8 * (i ^ ki) + 4 * wr + (r16 >> 3)) * 1024 + (4 * s + g) * 128 + (r16 & 7) * 16;
#pragma unroll
        for (int f = 0; f < 4; ++f) {
          boff[f][0] = K2_B + (8 * g + q4) * 256 + (((8 * ((f >> 1) ^ kj) + 4 * wc + 2 * (f & 1) + (p4 >> 1)) ^ (q4 << 2) ^ ((g & 1) << 1)) * 16) + 8 * (p4 & 1);
          boff[f][1] = boff[f][0] + 1024;
        }
      }
      SCHED();
      half(1, (t + 1) & 3);
    }
  }
#pragma unroll
  for (int t = 16; t < K2_NA; ++t) {
    SCHED();
    half(0, t & 3);
    BAR();
    SCHED();
    half(1, (t + 1) & 3);
  }
  STAMP(8);
  const int tile = mt * 4 + nt;
  const int voff = wid * 2048 + lane * 16;
#pragma unroll
  for (int s = 1; s < 4; ++s) {
    const __amdgpu_buffer_rsrc_t rp = mkrsrc(part + ((tile * 4 + (ksl ^ s)) * 3 + (s - 1)) * 8192);
#pragma unroll
    for (int a = 0; a < 2; ++a) {
      const f32x4 c0 = acc[(s & 2) + a][2 * (s & 1)], c1 = acc[(s & 2) + a][2 * (s & 1) + 1];
      const u32x4 v = {pack_bf16(c0[0], c0[1]), pack_bf16(c0[2], c0[3]), pack_bf16(c1[0], c1[1]), pack_bf16(c1[2], c1[3])};
      __builtin_amdgcn_raw_buffer_store_b128(v, rp, voff + a * 1024, 0, 16);
    }
  }
  STAMP(9);
  const __amdgpu_buffer_rsrc_t rq = mkrsrc(part + (tile * 4 + ksl) * 3 * 8192);
  const size_t obase = (size_t)(m0 + 64 * ki + 32 * wr + r16) * D_ + n0 + 64 * kj + 32 * wc + 16 * (g & 1) + 8 * (g >> 1);
  GLB_AS unsigned* mine = (GLB_AS unsigned*)(cnt + (tile * 4 + ksl) * 16);
  u32x4 xv[2], pv[3][2];
  unsigned seen = 0;
#pragma unroll
  for (int t = 0; t < NB; ++t) {
    SCHED();
    halfb(0, (K2_NA + t) & 3);
    if (t == SIG_T) { STAMP(10); WAIT_V(0); STAMP(11); }
    if (t == POLL_T && wid == 0) {
      STAMP(4);
      for (int spin = 0; spin < (1 << 22) && __builtin_amdgcn_readfirstlane(seen) < 3u; ++spin) {
        __builtin_amdgcn_s_sleep(1);
        seen = __hip_atomic_load(mine, __ATOMIC_RELAXED, __HIP_MEMORY_SCOPE_AGENT);
      }
      STAMP(5);
    }
    BAR();
    if (t == SIG_T && wid == 0 && lane >= 1 && lane < 4)
      __hip_atomic_fetch_add((GLB_AS unsigned*)(cnt + (tile * 4 + (ksl ^ lane)) * 16), 1u, __ATOMIC_RELAXED, __HIP_MEMORY_SCOPE_AGENT);
    if (t == XL_T) {
#pragma unroll
      for (int a = 0; a < 2; ++a) xv[a] = *(const u32x4*)(x + obase + (size_t)16 * D_ * a);
    }
    if (t == POLL_T - POLL_D && wid == 0) seen = __hip_atomic_load(mine, __ATOMIC_RELAXED, __HIP_MEMORY_SCOPE_AGENT);
    if (t == POLL_T) {
#pragma unroll
      for (int s = 0; s < 3; ++s)
#pragma unroll
        for (int a = 0; a < 2; ++a)
          pv[s][a] = __builtin_bit_cast(u32x4, __builtin_amdgcn_raw_buffer_load_b128(rq, voff + a * 1024 + s * 8192, 0, 16));
    }
    SCHED();
    halfb(1, (K2_NA + t + 1) & 3);
  }
  STAMP(6);
#pragma unroll
  for (int a = 0; a < 2; ++a) {
    const auto x0 = __builtin_amdgcn_permlane16_swap(xv[a][0], xv[a][2], false, false);
    const auto x1 = __builtin_amdgcn_permlane16_swap(xv[a][1], xv[a][3], false, false);
    const unsigned xx[4] = {x0[0], x1[0], x0[1], x1[1]};
    unsigned o[4];
#pragma unroll
    for (int e = 0; e < 4; ++e) {
      const unsigned p0 = pv[0][a][e], p1 = pv[1][a][e], p2 = pv[2][a][e];
      const float lo = ((acc[a][e >> 1][2 * (e & 1)] + bf_lo(p0)) + (bf_lo(p1) + bf_lo(p2))) + bf_lo(xx[e]);
      const float hi = ((acc[a][e >> 1][2 * (e & 1) + 1] + bf_hi(p0)) + (bf_hi(p1) + bf_hi(p2))) + bf_hi(xx[e]);
      o[e] = pack_bf16(lo, hi);
    }
    const auto r0 = __builtin_amdgcn_permlane16_swap(o[0], o[2], false, false);
    const auto r1 = __builtin_amdgcn_permlane16_swap(o[1], o[3], false, false);
    *(u32x4*)(out + obase + (size_t)16 * D_ * a) = u32x4{r0[0], r1[0], r0[1], r1[1]};
  }
  STAMP(7);
  STAMP(12);
  STAMP_FLUSH(1);
  return;
  }
  {
    __builtin_amdgcn_s_setprio(3);
    const int lw = wid - 4;
    const int l3 = lane >> 3, l7 = lane & 7;
    unsigned va[3][4], vb[3][4];
#pragma unroll
    for (int i = 0; i < 4; ++i) {
      va[0][i] = ((m0 + 32 * lw + 8 * i + l3) * 4096 + ((l7 ^ (g & 3)) * 16)) ^ ((i & 1) << 6);
      vb[0][i] = ((2 * lw + (i >> 1)) * 64 + n0 / 8 + 8 * (i & 1) + l3) * 128 + ((l7 ^ (4 * (i >> 1))) * 16);
    }
    int lbuf = 0;
    auto stage = [&](const unsigned (&oa)[4], const unsigned (&ob)[4], const char* pa, const char* pb) {
      char* lb = smem + lbuf * 16384 + lw * 4096;
      lbuf = (lbuf + 1) & 3;
      const __amdgpu_buffer_rsrc_t ra = mkrsrc(pa), rb = mkrsrc(pb);
#pragma unroll
      for (int i = 0; i < 4; ++i) blds16(rb, ob[i], lb + K2_B + i * 1024);
#pragma unroll
      for (int i = 0; i < 4; ++i) blds16(ra, oa[i], lb + i * 1024);
    };
    auto run = [&](int n, const unsigned (&oa)[4], const unsigned (&ob)[4], const char* pa, const char* pb, int sa, int sb) {
#pragma unroll 1
      for (int t = 0; t < n; ++t, pa += sa, pb += sb) {
        WAIT_V(8);
        BAR();
        stage(oa, ob, pa, pb);
      }
    };
    int ks = ROT2 == 1 ? (4 * nt + 2 * (idx >> 2)) & 15 : ROT2 == 2 ? (8 * ((idx >> 2) & 1)) : ROT2 == 3 ? (4 * ((idx >> 2) & 3)) : 0;
    auto stage0 = [&]() {
      stage(va[0], vb[0], (const char*)((ks & 8) ? wsim : wsre) + 1024 * ksl + 128 * (ks & 7),
            (const char*)ab + ksl * 524288 + (ks >> 3) * 2097152 + (ks & 7) * 65536);
      ks = (ks + 1) & 15;
    };
    stage0();
    stage0();
    asm volatile("s_waitcnt lgkmcnt(0)" ::"s"(kd0), "s"(kd1));
    STAMP(1);
    WAIT_V(8);
    BAR();
    stage0();
    SCHED();
#pragma unroll
    for (int i = 0; i < 4; ++i) {
      const int c = r16 ^ (g << 2) ^ ((i >> 1) << 1);
      va[1][i] = ((m0 / 8 + 4 * lw + i) * (DFF / 8) + l3) * 128 + l7 * 16;
      vb[1][i] = (16 * lw + 4 * i + g) * 1024 + n0 * 2 + c * 16;
      va[2][i] = ((m0 / 8 + 8 * ki + 4 * (lw & 1) + i) * (DFF / 8) + 8 * (lw >> 1) + l3) * 128 + l7 * 16;
      vb[2][i] = (64 * ((c >> 3) ^ kj ^ ki) + 16 * lw + 4 * i + g) * 1024 + (n0 + 64 * kj) * 2 + (c & 7) * 16;
    }
#pragma unroll
    for (int i = 0; i < 4; ++i) asm volatile("" : "+v"(va[1][i]), "+v"(vb[1][i]), "+v"(va[2][i]), "+v"(vb[2][i]));
#pragma unroll 1
    for (int t = 0; t < 13; ++t) {
      WAIT_V(8);
      BAR();
      stage0();
    }
    STAMP(4);
    run(NA, va[1], vb[1], (const char*)h + 1024 * NA * ksl, (const char*)w2 + (size_t)65536 * NA * ksl, 1024, 65536);
    STAMP(5);
    {
      const int kb = ROT3 ? (2 * nt + (idx >> 2)) % NB : 0;
      run(NB - kb, va[2], vb[2], (const char*)h + KB0 * 16 + kb * 2048, (const char*)w2 + (size_t)KB0 * 1024 + kb * 131072, 2048, 131072);
      run(kb, va[2], vb[2], (const char*)h + KB0 * 16, (const char*)w2 + (size_t)KB0 * 1024, 2048, 131072);
    }
    STAMP(6);
    WAIT_V(8);
    BAR();
    WAIT_V(0);
    BAR();
    BAR();
    STAMP(8);
    STAMP_FLUSH(1);
  }
}

extern "C" void kernel_launch(void* const* d_in, const int* in_sizes, int n_in, void* d_out, int out_size,
                              void* d_ws, size_t ws_size, hipStream_t stream) {
  const u16* x = (const u16*)d_in[0];
  const u16* wsre = (const u16*)d_in[1];
  const u16* wsim = (const u16*)d_in[2];
  const u16* wdre = (const u16*)d_in[3];
  const u16* wdim = (const u16*)d_in[4];
  const u16* w1 = (const u16*)d_in[6];
  const u16* w2 = (const u16*)d_in[7];
  char* ws = (char*)d_ws;
  unsigned* cnt = (unsigned*)(ws + WS_CNT);
  u16* ab = (u16*)(ws + WS_AB);
  u16* h = (u16*)(ws + WS_H);
  k1_ln_gemm1<<<256, 512, 0, stream>>>(x, wdre, wdim, w1, ab, h, cnt, wsre, wsim, w2, (const u16*)d_out);
  k2_gemm2<<<256, 512, 0, stream>>>(wsre, wsim, h, ab, w2, x, (u16*)d_out, ws + WS_P, cnt);
}
```

```cpp
#include <hip/hip_runtime.h>
#include <hip/hip_bf16.h>
#include <stdint.h>

typedef __attribute__((ext_vector_type(8))) short bf16x8;
typedef __attribute__((ext_vector_type(4))) short s16x4;
typedef __attribute__((ext_vector_type(2))) short s16x2;
typedef __attribute__((ext_vector_type(4))) float f32x4;
typedef __attribute__((ext_vector_type(2))) float f32x2;
typedef __attribute__((ext_vector_type(2))) __bf16 bf16x2_t;
typedef __attribute__((ext_vector_type(4))) unsigned u32x4;
typedef __attribute__((ext_vector_type(2))) unsigned u32x2;
typedef __attribute__((ext_vector_type(4))) unsigned long long u64x4;
typedef unsigned short u16;

#define LDS_AS __attribute__((address_space(3)))
#define GLB_AS __attribute__((address_space(1)))
#define WAIT_V(n) asm volatile("s_waitcnt vmcnt(%0)" ::"n"(n) : "memory")
#define WAIT_L0() asm volatile("s_waitcnt lgkmcnt(0)" ::: "memory")
#define WAIT_L(n) __builtin_amdgcn_s_waitcnt(0xC07F | ((n) << 8))
#define BAR() do { asm volatile("" ::: "memory"); __builtin_amdgcn_s_barrier(); asm volatile("" ::: "memory"); } while (0)
#define SCHED() __builtin_amdgcn_sched_barrier(0)

#define STAMP_DECL
#define STAMP(i)
#define RSTAMP(i)
#define STAMP_FLUSH(k)

#define BLK_LAYOUT 1
constexpr int S_ = 2048, D_ = 512, DFF = 1792;
constexpr int HLD = DFF;

constexpr size_t WS_CNT = 0;
constexpr size_t WS_AB = 65536;
constexpr size_t WS_H = WS_AB + (size_t)4096 * 512 * 2;
constexpr size_t WS_P = WS_H + (size_t)2048 * 2048 * 2;

#define TSL 7, 6
#define NA 5
#define SIG_T 1
#define POLL_T 3
#define POLL_D 1
#define XL_T (POLL_T - 2)
#define ROT1 0
#define ROT2 2
#define ROT3 1
#define CPF1 1
#define CPF2 1
#define TPAT 2
#define ST0 (2 + (idx % 5))
#define ST1 7
#define K1CL 160
#define EARLY 1
#define EARLY2 1
#define EARLY_N 16
#define BALC 1
#define PRE 1
#define PGT 1
#define TPOL ""
#define TOUCH64(d, p) asm volatile("global_load_dword %0, %1, off" TPOL : "+v"(d) : "v"(p) : "memory")
#define TOUCH(d, off, base) asm volatile("s_nop 4\n\tglobal_load_dword %0, %1, %2" : "+v"(d) : "v"(off), "s"(base) : "memory")

__device__ __forceinline__ void wait_v(int n) {
  switch (n) {
#define WV(k) case k: WAIT_V(k); break;
    WV(0) WV(1) WV(2) WV(3) WV(4) WV(5) WV(6) WV(10) WV(11) WV(12) WV(13) WV(14) WV(15) WV(16)
#undef WV
    default: WAIT_V(0);
  }
}
__device__ __forceinline__ float bf16_round(float f) { return __bfloat162float(__float2bfloat16(f)); }
__device__ __forceinline__ unsigned pack_bf16(float lo, float hi) {
  f32x2 v = {lo, hi};
  return __builtin_bit_cast(unsigned, __builtin_convertvector(v, bf16x2_t));
}
__device__ __forceinline__ float bf_lo(unsigned u) { return __builtin_bit_cast(float, u << 16); }
__device__ __forceinline__ float bf_hi(unsigned u) { return __builtin_bit_cast(float, u & 0xffff0000u); }
__device__ __forceinline__ __amdgpu_buffer_rsrc_t mkrsrc(const void* p) {
  return __builtin_amdgcn_make_buffer_rsrc((void*)p, 0, 0x7fffffff, 0x00020000);
}
__device__ __forceinline__ void blds16(__amdgpu_buffer_rsrc_t r, unsigned voff, char* l) {
  __builtin_amdgcn_raw_ptr_buffer_load_lds(r, (LDS_AS void*)l, 16, voff, 0, 0, 0);
}
__device__ __forceinline__ bf16x8 tr_read2(const char* p0, const char* p1) {
  s16x4 t0 = __builtin_amdgcn_ds_read_tr16_b64_v4i16((LDS_AS s16x4*)(p0));
  s16x4 t1 = __builtin_amdgcn_ds_read_tr16_b64_v4i16((LDS_AS s16x4*)(p1));
  return __builtin_shufflevector(t0, t1, 0, 1, 2, 3, 4, 5, 6, 7);
}
#define MFMA16(a, b, c) __builtin_amdgcn_mfma_f32_16x16x32_bf16(a, b, c, 0, 0, 0)
__device__ __forceinline__ bf16x8 split3(float c, bool z) {
  const float hi = bf16_round(c), r1 = c - hi, mid = bf16_round(r1), lo = r1 - mid;
  u32x4 v = {pack_bf16(hi, mid), pack_bf16(lo, 0.f), 0u, 0u};
  if (z) v = u32x4{0u, 0u, 0u, 0u};
  return __builtin_bit_cast(bf16x8, v);
}

constexpr int K1_W = 65536, K1_WS = 24576;
constexpr int K1_CODE_LINES = K1CL, K2_CODE_LINES = 84;
constexpr int KB0 = 4 * NA * 64;
constexpr int NB = (DFF - KB0) / 128;

struct Tile { bool nc, im, skip; int col0; };
__device__ __forceinline__ Tile tile_of(int bid) {
  const int ntile = 4 * ((bid & 7) >> 1) + ((bid >> 3) & 3);
  const unsigned long long G = BALC ? 0ull | 3ull << 6 | 37ull << 12 | 0ull << 18 | 3ull << 24 | 37ull << 30 | 0ull << 36 | 5ull << 42 | 34ull << 48 | 8ull << 54
                                    : 0ull | 3ull << 6 | 37ull << 12 | 0ull << 18 | 3ull << 24 | 37ull << 30 | 0ull << 36 | 34ull << 42 | 5ull << 48 | 8ull << 54;
  const unsigned long long H = 11ull | 14ull << 6 | 48ull << 12 | 19ull << 18 | 22ull << 24 | 25ull << 30;
  const unsigned e = (unsigned)((ntile < 10 ? G : H) >> (6 * (ntile < 10 ? ntile : ntile - 10)));
  return Tile{ntile >= 6, ntile >= 3, (e & 32) != 0, (int)(e & 31) * 64};
}

__device__ __forceinline__ void k1_loader(char* smem, int lw, u64x4 ka, int m0,
                                          const u16* pfa, const u16* pfb, const u16* pfc, const char* code2,
                                          const char* wsb, const char* outb, unsigned kd1, unsigned pfd, unsigned cf1) {
  const int tid = threadIdx.x, lane = tid & 63;
  STAMP_DECL
  RSTAMP(14);
  STAMP(0);
  const int xcd = blockIdx.x & 7, idx = blockIdx.x >> 3;
  unsigned vx[4], vw[6];
#pragma unroll
  for (int i = 0; i < 4; ++i)
    vx[i] = ((m0 + 32 * lw + 8 * i + (lane >> 3)) * (D_ * 2) + (((lane & 7) ^ ((lane >> 4) & 3)) * 16)) ^ ((i & 1) << 6);
  const char *x, *kp1, *kp2, *kp3;
  const char* wb;
  int wstep;
  auto stage_x = [&](int t) {
    const __amdgpu_buffer_rsrc_t px = mkrsrc((const char*)x + t * 128);
#pragma unroll
    for (int i = 0; i < 4; ++i) blds16(px, vx[i], smem + (t & 3) * 16384 + (4 * lw + i) * 1024);
  };
  auto stage_w = [&](int t) {
    const __amdgpu_buffer_rsrc_t pw = mkrsrc(wb + t * wstep);
#pragma unroll
    for (int i = 0; i < 6; ++i) blds16(pw, vw[i], smem + K1_W + (t & 3) * K1_WS + (6 * lw + i) * 1024);
  };
  SCHED();
  int b2 = blockIdx.x;
  asm volatile("" : "+s"(b2));
  const Tile tl = tile_of(b2);
  const bool nc = tl.nc;
  wstep = nc ? 64 * (DFF * 2) : 128;
  __builtin_amdgcn_s_setprio(3);
  if (nc) {
#pragma unroll
    for (int i = 0; i < 3; ++i) {
      const int id = 64 * i + lane, kr = id / 24, c = id % 24;
      vw[i] = (16 * lw + kr) * (DFF * 2) + ((c ^ (((kr >> 1) & 1) << 2)) * 16);
      vw[i + 3] = (vw[i] ^ 32) + 8 * (DFF * 2);
    }
  } else {
#pragma unroll
    for (int i = 0; i < 6; ++i)
      vw[i] = ((48 * lw + 8 * i + (lane >> 3)) * (D_ * 2) + (((lane & 7) ^ ((lane >> 4) & 3)) * 16)) ^ ((i & 1) << 6);
  }
#pragma unroll
  for (int i = 0; i < 6; ++i) asm volatile("" : "+v"(vw[i]));
  int wo = nc ? tl.col0 * 2 : tl.col0 * (D_ * 2);
  asm volatile("" : "+s"(wo));
  asm volatile("" : "+v"(vx[0]), "+v"(vx[1]), "+v"(vx[2]), "+v"(vx[3]));
  asm volatile("s_waitcnt lgkmcnt(0)\n\ts_mov_b64 %0, %4\n\ts_mov_b64 %1, %5\n\ts_mov_b64 %2, %6\n\ts_mov_b64 %3, %7"
               : "=&s"(x), "=&s"(kp1), "=&s"(kp2), "=&s"(kp3) : "s"(ka[0]), "s"(ka[1]), "s"(ka[2]), "s"(ka[3]), "s"(kd1));
  stage_x(0);
  wb = (nc ? kp3 : tl.im ? kp2 : kp1) + wo;
  stage_w(0);
  stage_x(1);
  stage_w(1);
  SCHED();
  const char* ta[4];
  {
    const int ksl = xcd >> 1, t2 = tid - 256;
    constexpr int LA = 2 * NA * 8, LB = (DFF - KB0) / 32 * 4;
    const int c = (2 * (idx >> 2) + 4 * (t2 & 1) + ((t2 >> 1) & 1)) & 7;
    ta[0] = (const char*)((t2 & 4) ? pfb : pfa) + (1024 * (xcd & 1) + 32 * idx + (t2 >> 3)) * 4096 + 1024 * ksl + c * 128;
    ta[1] = (const char*)((t2 & 4) ? pfb : pfa) + (1024 * (xcd & 1) + 32 * idx + (t2 >> 3)) * 4096 + 1024 * ksl + (c ^ 2) * 128;
    ta[0] = (const char*)((t2 & 128) ? pfb : pfa) + (1024 * (xcd & 1) + 32 * idx + ((t2 >> 3) & 15)) * 4096 + 1024 * ksl + (t2 & 7) * 128;
    ta[1] = ta[0] + 16 * 4096;
    const int tb = min(max(t2 - LA, 0), LB - 1);
    ta[2] = (const char*)pfc + (t2 < LA ? (64 * NA * ksl + 2 * NA * idx + (t2 >> 3)) * 1024 + (t2 & 7) * 128
                                        : (KB0 + (LB / 4) * idx + (tb >> 2)) * 1024 + (tb & 3) * 256 + (ksl & 1) * 128);
    const int j = min(11 * lw + min(lane, 10), 43);
    ta[3] = j < 37 ? wsb + 32768 + j * 524288 : j < 41 ? outb + (j - 37) * 524288
                   : code2 + min(3 * idx + j - 41, K2_CODE_LINES - 1) * 128;
  }
  constexpr int TP[2] = {TSL};
  const int tp0 = ST0, tp1 = ST1;
  auto ntouch = [&](int sg) { return (TP[0] == sg) + (TP[1] == sg); };
  asm volatile("" : "+v"(ta[0]), "+v"(ta[1]), "+v"(ta[2]), "+v"(ta[3]));
  STAMP(1);
  WAIT_V(10);
  asm volatile("" ::"v"(cf1));
  BAR();
  STAMP(2);
  stage_x(2);
  stage_w(2);
  if (tp0 == 2) TOUCH64(pfd, ta[0]);
#pragma unroll
  for (int t = 0; t < 8; ++t) {
    if (t < 7) {
      const int n = t + 1 < 7 ? ntouch(t + 1) + 10 + ntouch(t + 2) : ntouch(7);
      const int d = (tp0 == t + 1) + (tp0 == t + 2) + (tp1 == t + 1) + (tp1 == t + 2);
      if (d == 0) wait_v(n);
      else if (d == 1) wait_v(n + 1);
      else wait_v(n + 2);
    }
    BAR();
    if (t + 3 < 8) {
      stage_x(t + 3);
      stage_w(t + 3);
      if (TP[1] == t + 3) TOUCH64(pfd, ta[3]);
      if (tp0 == t + 3) TOUCH64(pfd, ta[0]);
      if (tp1 == t + 3) TOUCH64(pfd, ta[1]);
      if (TP[0] == t + 3) TOUCH64(pfd, ta[2]);
    }
  }
  STAMP(3);
  BAR();
  WAIT_V(0);
  asm volatile("" ::"v"(pfd));
  STAMP(12);
  STAMP_FLUSH(0);
}

template <bool NC>
__device__ __forceinline__ void k1_body(char* smem, int wid, u16* __restrict__ dst, int m0, int col0, bool skip64, float sgn,
                                        u64x4 ka, unsigned kd1, unsigned cf0, unsigned cf1) {
  const int tid = threadIdx.x, lane = tid & 63;
  STAMP_DECL
  RSTAMP(14);
  STAMP(0);
  float* stat = (float*)smem;
  const int wr = wid >> 1, wc = wid & 1;
  const int r16 = lane & 15, g = lane >> 4;
  int aoff[2][2], woff[2][4];
#pragma unroll
  for (int s = 0; s < 2; ++s)
#pragma unroll
    for (int e = 0; e < 2; ++e)
      aoff[s][e] = (64 * wr + 16 * (e ^ wc) + r16) * 128 + (((4 * s + g) ^ (r16 >> 1)) * 16);
#pragma unroll
  for (int j = 0; j < 4; ++j) {
    if constexpr (NC) {
      const int q = r16 >> 2, p = lane & 3;
      woff[0][j] = K1_W + (8 * g + q) * 384 + (((6 * wc + (j ^ wr)) ^ (q & 2) ^ (g & 1)) * 32) + p * 8;
    } else {
      woff[0][j] = K1_W + (96 * wc + 16 * ((j & 1) ^ wr) + r16) * 128 + (((4 * (j >> 1) + g) ^ (r16 >> 1)) * 16);
    }
    woff[1][j] = woff[0][j] + 2 * K1_WS;
    asm volatile("" : "+v"(woff[1][j]));
  }
  const f32x4 z4 = {0.f, 0.f, 0.f, 0.f};
  f32x4 acc[4][6], s1[2] = {z4, z4}, s2[2] = {z4, z4}, cs[3] = {z4, z4, z4};
#pragma unroll
  for (int v = 0; v < 6; ++v)
#pragma unroll
    for (int u = 0; u < 4; ++u) acc[u][v] = z4;
  bf16x8 ones;
#pragma unroll
  for (int e = 0; e < 8; ++e) ones[e] = (short)0x3F80;
  bf16x8 xa[2][4];
  s16x4 wf[2][6][2];
  auto rdn = [&](int set, int b, int n) {
    constexpr int TX[16] = {-1, -1, 0, 1, -1, -1, 2, -1, -1, 3, -1, -1, -1, -1, -1, -1};
    constexpr int TW[16] = {0, 1, -1, -1, 2, 3, -1, 4, 5, -1, 6, 7, 8, 9, 10, 11};
    if (TX[n] >= 0) {
      const int u = TX[n];
      xa[set][u] = *(const bf16x8*)(smem + aoff[set][u & 1] + (u >> 1) * 4096 + b * 16384);
    } else if constexpr (NC) {
      const int v = TW[n] >> 1, hi = TW[n] & 1;
      wf[set][v][hi] = __builtin_amdgcn_ds_read_tr16_b64_v4i16(
          (LDS_AS s16x4*)(smem + woff[b >> 1][v & 3] + (b & 1) * K1_WS + set * 12288 + hi * 1536 + (v >> 2) * 128));
    } else if (!(TW[n] & 1)) {
      const int v = TW[n] >> 1;
      const bf16x8 t = *(const bf16x8*)(smem + woff[b >> 1][2 * set + (v & 1)] + (b & 1) * K1_WS + (v >> 1) * 4096);
      wf[set][v][0] = __builtin_shufflevector(t, t, 0, 1, 2, 3);
      wf[set][v][1] = __builtin_shufflevector(t, t, 4, 5, 6, 7);
    }
  };
#define WF(set, v) __builtin_shufflevector(wf[set][v][0], wf[set][v][1], 0, 1, 2, 3, 4, 5, 6, 7)
  auto half = [&](int set, int b) {
    constexpr int OV[24] = {0, 0, 1, 1, 0, 1, 2, 2, 2, 0, 1, 2, 3, 3, 3, 3, 4, 4, 4, 4, 5, 5, 5, 5};
    constexpr int OU[24] = {0, 1, 0, 1, 2, 2, 0, 1, 2, 3, 3, 3, 0, 1, 2, 3, 0, 1, 2, 3, 0, 1, 2, 3};
    WAIT_L(NC ? 6 : 3);
#pragma unroll
    for (int n = 0; n < 24; ++n) {
      acc[OU[n]][OV[n]] = MFMA16(WF(set, OV[n]), xa[set][OU[n]], acc[OU[n]][OV[n]]);
      SCHED();
      if (n < 16) rdn(set ^ 1, b, n);
      if (n == 2 || n == 8) s1[n / 6] = MFMA16(ones, xa[set][2 * (n / 6)], s1[n / 6]);
      if (n == 5 || n == 11) s2[n / 6] = MFMA16(xa[set][2 * (n / 6)], xa[set][2 * (n / 6)], s2[n / 6]);
      if (n == 14 || n == 17 || n == 20) cs[(n - 14) / 3] = MFMA16(ones, WF(set, 2 * ((n - 14) / 3)), cs[(n - 14) / 3]);
      SCHED();
    }
  };
#pragma unroll
  for (int v = 0; v < 6; ++v) asm volatile("" : "+v"(acc[0][v]), "+v"(acc[1][v]), "+v"(acc[2][v]), "+v"(acc[3][v]));
  asm volatile("" : "+v"(s1[0]), "+v"(s1[1]), "+v"(s2[0]), "+v"(s2[1]), "+v"(cs[0]), "+v"(cs[1]), "+v"(cs[2]), "+v"(ones));
  asm volatile("s_waitcnt vmcnt(0) lgkmcnt(0)" ::"s"(ka), "s"(kd1), "v"(cf0), "v"(cf1));
  BAR();
  STAMP(2);
#pragma unroll
  for (int n = 0; n < 16; ++n) rdn(0, 0, n);
#pragma unroll 1
  for (int it = 0; it < 2; ++it) {
#pragma unroll
    for (int t = 0; t < 4; ++t) {
      SCHED();
      half(0, t);
      BAR();
      SCHED();
      half(1, (t + 1) & 3);
    }
  }
  STAMP(3);
  if (g == 0) {
#pragma unroll
    for (int w = 0; w < 3; ++w) stat[256 + 96 * wc + 16 * (2 * w + wr) + r16] = cs[w][0];
  }
  if (g == (r16 >> 2)) {
#pragma unroll
    for (int rg = 0; rg < 2; ++rg) {
      const float d = (lane & 2) ? ((lane & 1) ? s2[rg][3] : s2[rg][2]) : ((lane & 1) ? s2[rg][1] : s2[rg][0]);
      const float mean = s1[rg][0] * (1.f / 512.f);
      const float var = fmaxf(d * (1.f / 512.f) - mean * mean, 0.f);
      const float den = bf16_round(__builtin_amdgcn_sqrtf(bf16_round(bf16_round(var) + 1e-5f)));
      *(f32x2*)(stat + 2 * (64 * wr + 16 * (2 * rg + wc) + r16)) = f32x2{-mean, sgn * __builtin_amdgcn_rcpf(den)};
    }
  }
  WAIT_L0();
  BAR();
  STAMP(4);
  f32x2 st[4];
  float co[3];
#pragma unroll
  for (int u = 0; u < 4; ++u) st[u] = *(const f32x2*)(stat + 2 * (64 * wr + 16 * (u ^ wc) + r16));
#pragma unroll
  for (int w = 0; w < 3; ++w) co[w] = stat[256 + 96 * wc + 16 * (2 * w + (wr ^ 1)) + r16];
  bf16x8 xm[4];
#pragma unroll
  for (int u = 0; u < 4; ++u) {
    const unsigned nm = pack_bf16(st[u][0], st[u][0]);
    xm[u] = __builtin_bit_cast(bf16x8, u32x4{nm, nm & 0xffffu, 0u, 0u});
  }
  constexpr int NCH = NC ? DFF / 8 : D_ / 8;
  const __amdgpu_buffer_rsrc_t ro = mkrsrc((const char*)dst + ((size_t)(m0 >> 3) * NCH + col0 / 8) * 128);
  unsigned vo[4];
#pragma unroll
  for (int u = 0; u < 4; ++u)
    vo[u] = ((8 * wr + 2 * (u ^ wc) + (r16 >> 3)) * NCH + 12 * wc + 2 * ((g & 1) ^ wr) + (g >> 1)) * 128 + (r16 & 7) * 16;
#pragma unroll
  for (int w = 0; w < 3; ++w) {
    if (skip64 && wc == 0 && w < 2) continue;
#pragma unroll
    for (int s = 0; s < 2; ++s) {
      const bf16x8 wm = split3(s ? co[w] : cs[w][0], g != 0);
#pragma unroll
      for (int u = 0; u < 4; ++u) acc[u][2 * w + s] = MFMA16(wm, xm[u], acc[u][2 * w + s]);
    }
#pragma unroll
    for (int u = 0; u < 4; ++u) {
      unsigned pk[2][2];
#pragma unroll
      for (int s = 0; s < 2; ++s)
#pragma unroll
        for (int e = 0; e < 2; ++e) {
          unsigned t = pack_bf16(acc[u][2 * w + s][2 * e] * st[u][1], acc[u][2 * w + s][2 * e + 1] * st[u][1]);
          if constexpr (NC) t = __builtin_bit_cast(unsigned, __builtin_elementwise_max(__builtin_bit_cast(s16x2, t), s16x2{0, 0}));
          pk[s][e] = t;
        }
      const auto r0 = __builtin_amdgcn_permlane16_swap(pk[0][0], pk[1][0], false, false);
      const auto r1 = __builtin_amdgcn_permlane16_swap(pk[0][1], pk[1][1], false, false);
      __builtin_amdgcn_raw_buffer_store_b128(u32x4{r0[0], r1[0], r0[1], r1[1]}, ro, vo[u] + w * 512, 0, 16);
    }
  }
  STAMP(6);
  STAMP(7);
  STAMP_FLUSH(0);
}

__global__ __launch_bounds__(512, 2) void k1_ln_gemm1(const u16* __restrict__ x, const u16* __restrict__ wdre,
                                                      const u16* __restrict__ wdim, const u16* __restrict__ w1,
                                                      u16* __restrict__ ab, u16* __restrict__ h,
                                                      unsigned* __restrict__ cnt, const u16* __restrict__ wsre,
                                                      const u16* __restrict__ wsim, const u16* __restrict__ w2,
                                                      const u16* outp) {
  __shared__ __attribute__((aligned(1024))) char smem[K1_W + 4 * K1_WS];
  const char* code1;
  int cdist;
  u64x4 ka;
  unsigned kd1, cf0, cf1;
  asm volatile("s_getpc_b64 %0\nk1_code_mark:\n\ts_load_dwordx8 %2, %4, 0x0\n\ts_load_dword %3, %4, 0x40\n\ts_mov_b32 %1, k2_code_mark-k1_code_mark"
               : "=&s"(code1), "=&s"(cdist), "=&s"(ka), "=&s"(kd1) : "s"(__builtin_amdgcn_kernarg_segment_ptr()));
  asm volatile("v_and_b32 %0, 15, %2\n\tv_lshlrev_b32 %0, 7, %0\n\tglobal_load_dword %0, %0, %3\n\t"
               "v_and_b32 %1, 7, %2\n\tv_add_lshl_u32 %1, %1, %4, 7\n\tglobal_load_dword %1, %1, %3 offset:2048"
               : "=&v"(cf0), "=&v"(cf1) : "v"(threadIdx.x), "s"(code1), "s"(blockIdx.x & 0x78) : "memory");
  SCHED();
  const int wid = __builtin_amdgcn_readfirstlane(threadIdx.x >> 6);
  const int m0 = (8 * (blockIdx.x & 1) + (blockIdx.x >> 5)) * 128;
  if (wid < 4) {
    const Tile tl = tile_of(blockIdx.x);
    if (threadIdx.x == 0) cnt[blockIdx.x * 16] = 0;
    if (!tl.nc) k1_body<false>(smem, wid, ab + (size_t)tl.im * 2048 * D_, m0, tl.col0, tl.skip, tl.im ? -1.f : 1.f, ka, kd1, cf0, cf1);
    else k1_body<true>(smem, wid, h, m0, tl.col0, tl.skip, 1.f, ka, kd1, cf0, cf1);
    return;
  }
  k1_loader(smem, wid - 4, ka, m0, wsre, wsim, w2, code1 + cdist, (const char*)cnt, (const char*)outp, kd1, cf0, cf1);
}

constexpr int K2_NA = 16 + NA, K2_NT = K2_NA + NB, K2_B = 65536;

__global__ __launch_bounds__(512, 2) void k2_gemm2(const u16* __restrict__ wsre_, const u16* __restrict__ wsim_,
                                                   const u16* __restrict__ h_, const u16* __restrict__ ab_,
                                                   const u16* __restrict__ w2_, const u16* __restrict__ x,
                                                   u16* __restrict__ out, char* __restrict__ part,
                                                   unsigned* __restrict__ cnt) {
  __shared__ __attribute__((aligned(1024))) char smem[2 * K2_B];
  u64x4 ka;
  unsigned long long kw;
  unsigned kd1;
  asm volatile("k2_code_mark:\n\ts_load_dwordx8 %0, %3, 0x0\n\ts_load_dwordx2 %1, %3, 0x20\n\ts_load_dword %2, %3, 0x40"
               : "=&s"(ka), "=&s"(kw), "=&s"(kd1) : "s"(__builtin_amdgcn_kernarg_segment_ptr()));
  SCHED();
  const int tid = threadIdx.x, lane = tid & 63;
  const int wid = __builtin_amdgcn_readfirstlane(tid >> 6);
  const int bid = blockIdx.x, xcd = bid & 7, idx = bid >> 3;
  const int ksl = xcd >> 1, mt = 8 * (xcd & 1) + (idx >> 2), nt = idx & 3;
  const int ki = ksl >> 1, kj = ksl & 1;
  const int m0 = mt * 128, n0 = nt * 128;
  const int r16 = lane & 15, g = lane >> 4;
  STAMP_DECL
  RSTAMP(14);
  STAMP(0);
  if (wid < 4) {
  const int wr = wid >> 1, wc = wid & 1;
  const int q4 = r16 >> 2, p4 = lane & 3;
  int aoff[2][2], boff[4][2];
#pragma unroll
  for (int s = 0; s < 2; ++s)
#pragma unroll
    for (int i = 0; i < 2; ++i) aoff[s][i] = (64 * (i ^ ki) + 32 * wr + r16) * 128 + (((4 * s + g) ^ (r16 >> 1)) * 16);
#pragma unroll
  for (int f = 0; f < 4; ++f) {
    boff[f][0] = K2_B + g * 2048 + (8 * ((f >> 1) ^ kj) + 4 * wc + 2 * (f & 1) + (p4 >> 1)) * 128 + (q4 + 4 * (g & 1)) * 16 + 8 * (p4 & 1);
    boff[f][1] = boff[f][0] ^ 64;
  }
  const f32x4 z4 = {0.f, 0.f, 0.f, 0.f};
  f32x4 acc[4][4];
#pragma unroll
  for (int a = 0; a < 4; ++a)
#pragma unroll
    for (int b = 0; b < 4; ++b) acc[a][b] = z4;
  bf16x8 am[2][4];
  s16x4 bm[2][4][2];
  auto rdn = [&](int set, int b, int n) {
    constexpr int TA[12] = {-1, -1, 0, 1, -1, -1, 2, -1, -1, 3, -1, -1};
    constexpr int TB[12] = {0, 1, -1, -1, 2, 3, -1, 4, 5, -1, 6, 7};
    if (TA[n] >= 0) am[set][TA[n]] = *(const bf16x8*)(smem + aoff[set][TA[n] >> 1] + (TA[n] & 1) * 2048 + b * 16384);
    else
      bm[set][TB[n] >> 1][TB[n] & 1] = __builtin_amdgcn_ds_read_tr16_b64_v4i16(
          (LDS_AS s16x4*)(smem + boff[TB[n] >> 1][TB[n] & 1] + set * 8192 + b * 16384));
  };
#define BM(set, f) __builtin_shufflevector(bm[set][f][0], bm[set][f][1], 0, 1, 2, 3, 4, 5, 6, 7)
  auto half = [&](int set, int b) {
    constexpr int OB[16] = {0, 0, 1, 1, 0, 1, 2, 2, 2, 0, 1, 2, 3, 3, 3, 3};
    constexpr int OA[16] = {0, 1, 0, 1, 2, 2, 0, 1, 2, 3, 3, 3, 0, 1, 2, 3};
    WAIT_L(3);
#pragma unroll
    for (int n = 0; n < 16; ++n) {
      acc[OA[n]][OB[n]] = MFMA16(BM(set, OB[n]), am[set][OA[n]], acc[OA[n]][OB[n]]);
      SCHED();
      if (n < 12) rdn(set ^ 1, b, n);
      SCHED();
    }
  };
  auto halfb = [&](int set, int b) {
#pragma unroll
    for (int n = 0; n < 8; ++n) {
      const int i = n >> 2, bb = (n >> 1) & 1, a = n & 1;
      acc[a][bb] = MFMA16(BM(set, 2 * i + bb), am[set][2 * i + a], acc[a][bb]);
      SCHED();
      if (n < 6) { rdn(set ^ 1, b, 2 * n); rdn(set ^ 1, b, 2 * n + 1); }
      SCHED();
    }
  };
#pragma unroll
  for (int a = 0; a < 4; ++a) asm volatile("" : "+v"(acc[a][0]), "+v"(acc[a][1]), "+v"(acc[a][2]), "+v"(acc[a][3]));
  asm volatile("s_waitcnt lgkmcnt(0)" ::"s"(ka), "s"(kw), "s"(kd1));
  BAR();
  STAMP(2);
#pragma unroll
  for (int n = 0; n < 12; ++n) rdn(0, 0, n);
#pragma unroll 1
  for (int it = 0; it < 4; ++it) {
#pragma unroll
    for (int t = 0; t < 4; ++t) {
      SCHED();
      half(0, t);
      BAR();
      if (t == 3 && it == 3) {
#pragma unroll
        for (int s = 0; s < 2; ++s)
#pragma unroll
          for (int i = 0; i < 2; ++i) aoff[s][i] = (8 * (i ^ ki) + 4 * wr + (r16 >> 3)) * 1024 + (4 * s + g) * 128 + (r16 & 7) * 16;
#pragma unroll
        for (int f = 0; f < 4; ++f) {
          boff[f][0] = K2_B + (8 * g + q4) * 256 + (((8 * ((f >> 1) ^ kj) + 4 * wc + 2 * (f & 1) + (p4 >> 1)) ^ (q4 << 2) ^ ((g & 1) << 1)) * 16) + 8 * (p4 & 1);
          boff[f][1] = boff[f][0] + 1024;
        }
      }
      SCHED();
      half(1, (t + 1) & 3);
    }
  }
#pragma unroll
  for (int t = 16; t < K2_NA; ++t) {
    SCHED();
    half(0, t & 3);
    BAR();
    SCHED();
    half(1, (t + 1) & 3);
  }
  STAMP(8);
  const int tile = mt * 4 + nt;
  const int voff = wid * 2048 + lane * 16;
#pragma unroll
  for (int s = 1; s < 4; ++s) {
    const __amdgpu_buffer_rsrc_t rp = mkrsrc(part + ((tile * 4 + (ksl ^ s)) * 3 + (s - 1)) * 8192);
#pragma unroll
    for (int a = 0; a < 2; ++a) {
      const f32x4 c0 = acc[(s & 2) + a][2 * (s & 1)], c1 = acc[(s & 2) + a][2 * (s & 1) + 1];
      const u32x4 v = {pack_bf16(c0[0], c0[1]), pack_bf16(c0[2], c0[3]), pack_bf16(c1[0], c1[1]), pack_bf16(c1[2], c1[3])};
      __builtin_amdgcn_raw_buffer_store_b128(v, rp, voff + a * 1024, 0, 16);
    }
  }
  STAMP(9);
  const __amdgpu_buffer_rsrc_t rq = mkrsrc(part + (tile * 4 + ksl) * 3 * 8192);
  const size_t obase = (size_t)(m0 + 64 * ki + 32 * wr + r16) * D_ + n0 + 64 * kj + 32 * wc + 16 * (g & 1) + 8 * (g >> 1);
  GLB_AS unsigned* mine = (GLB_AS unsigned*)(cnt + (tile * 4 + ksl) * 16);
  u32x4 xv[2], pv[3][2];
  unsigned seen = 0;
#pragma unroll
  for (int t = 0; t < NB; ++t) {
    SCHED();
    halfb(0, (K2_NA + t) & 3);
    if (t == SIG_T) { STAMP(10); WAIT_V(0); STAMP(11); }
    if (t == POLL_T && wid == 0) {
      STAMP(4);
      for (int spin = 0; spin < (1 << 22) && __builtin_amdgcn_readfirstlane(seen) < 3u; ++spin) {
        __builtin_amdgcn_s_sleep(1);
        seen = __hip_atomic_load(mine, __ATOMIC_RELAXED, __HIP_MEMORY_SCOPE_AGENT);
      }
      STAMP(5);
    }
    BAR();
    if (t == SIG_T && wid == 0 && lane >= 1 && lane < 4)
      __hip_atomic_fetch_add((GLB_AS unsigned*)(cnt + (tile * 4 + (ksl ^ lane)) * 16), 1u, __ATOMIC_RELAXED, __HIP_MEMORY_SCOPE_AGENT);
    if (t == XL_T) {
#pragma unroll
      for (int a = 0; a < 2; ++a) xv[a] = *(const u32x4*)(x + obase + (size_t)16 * D_ * a);
    }
    if (t == POLL_T - POLL_D && wid == 0) seen = __hip_atomic_load(mine, __ATOMIC_RELAXED, __HIP_MEMORY_SCOPE_AGENT);
    if (t == POLL_T) {
#pragma unroll
      for (int s = 0; s < 3; ++s)
#pragma unroll
        for (int a = 0; a < 2; ++a)
          pv[s][a] = __builtin_bit_cast(u32x4, __builtin_amdgcn_raw_buffer_load_b128(rq, voff + a * 1024 + s * 8192, 0, 16));
    }
    SCHED();
    halfb(1, (K2_NA + t + 1) & 3);
  }
  STAMP(6);
#pragma unroll
  for (int a = 0; a < 2; ++a) {
    const auto x0 = __builtin_amdgcn_permlane16_swap(xv[a][0], xv[a][2], false, false);
    const auto x1 = __builtin_amdgcn_permlane16_swap(xv[a][1], xv[a][3], false, false);
    const unsigned xx[4] = {x0[0], x1[0], x0[1], x1[1]};
    unsigned o[4];
#pragma unroll
    for (int e = 0; e < 4; ++e) {
      const unsigned p0 = pv[0][a][e], p1 = pv[1][a][e], p2 = pv[2][a][e];
      const float lo = ((acc[a][e >> 1][2 * (e & 1)] + bf_lo(p0)) + (bf_lo(p1) + bf_lo(p2))) + bf_lo(xx[e]);
      const float hi = ((acc[a][e >> 1][2 * (e & 1) + 1] + bf_hi(p0)) + (bf_hi(p1) + bf_hi(p2))) + bf_hi(xx[e]);
      o[e] = pack_bf16(lo, hi);
    }
    const auto r0 = __builtin_amdgcn_permlane16_swap(o[0], o[2], false, false);
    const auto r1 = __builtin_amdgcn_permlane16_swap(o[1], o[3], false, false);
    *(u32x4*)(out + obase + (size_t)16 * D_ * a) = u32x4{r0[0], r1[0], r0[1], r1[1]};
  }
  STAMP(7);
  STAMP(12);
  STAMP_FLUSH(1);
  return;
  }
  {
    __builtin_amdgcn_s_setprio(3);
    const int lw = wid - 4;
    const int l3 = lane >> 3, l7 = lane & 7;
    unsigned va[3][4], vb[3][4];
#pragma unroll
    for (int i = 0; i < 4; ++i) {
      va[0][i] = ((m0 + 32 * lw + 8 * i + l3) * 4096 + ((l7 ^ (g & 3)) * 16)) ^ ((i & 1) << 6);
      vb[0][i] = ((2 * lw + (i >> 1)) * 64 + n0 / 8 + 8 * (i & 1) + l3) * 128 + ((l7 ^ (4 * (i >> 1))) * 16);
    }
#pragma unroll
    for (int i = 0; i < 4; ++i) asm volatile("" : "+v"(va[0][i]), "+v"(vb[0][i]));
    const char *wsre, *wsim, *h, *ab, *w2;
    asm volatile("s_waitcnt lgkmcnt(0)\n\ts_mov_b64 %0, %5\n\ts_mov_b64 %1, %6\n\ts_mov_b64 %2, %7\n\ts_mov_b64 %3, %8\n\ts_mov_b64 %4, %9"
                 : "=&s"(wsre), "=&s"(wsim), "=&s"(h), "=&s"(ab), "=&s"(w2) : "s"(ka[0]), "s"(ka[1]), "s"(ka[2]), "s"(ka[3]), "s"(kw), "s"(kd1));
    int lbuf = 0;
    auto stage = [&](const unsigned (&oa)[4], const unsigned (&ob)[4], const char* pa, const char* pb) {
      char* lb = smem + lbuf * 16384 + lw * 4096;
      lbuf = (lbuf + 1) & 3;
      const __amdgpu_buffer_rsrc_t ra = mkrsrc(pa), rb = mkrsrc(pb);
#pragma unroll
      for (int i = 0; i < 4; ++i) blds16(rb, ob[i], lb + K2_B + i * 1024);
#pragma unroll
      for (int i = 0; i < 4; ++i) blds16(ra, oa[i], lb + i * 1024);
    };
    auto run = [&](int n, const unsigned (&oa)[4], const unsigned (&ob)[4], const char* pa, const char* pb, int sa, int sb) {
#pragma unroll 1
      for (int t = 0; t < n; ++t, pa += sa, pb += sb) {
        WAIT_V(8);
        BAR();
        stage(oa, ob, pa, pb);
      }
    };
    int ks = ROT2 == 1 ? (4 * nt + 2 * (idx >> 2)) & 15 : ROT2 == 2 ? (8 * ((idx >> 2) & 1)) : ROT2 == 3 ? (4 * ((idx >> 2) & 3)) : 0;
    auto stage0 = [&]() {
      stage(va[0], vb[0], (const char*)((ks & 8) ? wsim : wsre) + 1024 * ksl + 128 * (ks & 7),
            (const char*)ab + ksl * 524288 + (ks >> 3) * 2097152 + (ks & 7) * 65536);
      ks = (ks + 1) & 15;
    };
    stage0();
    stage0();
    STAMP(1);
    WAIT_V(8);
    BAR();
    stage0();
    SCHED();
#pragma unroll
    for (int i = 0; i < 4; ++i) {
      const int c = r16 ^ (g << 2) ^ ((i >> 1) << 1);
      va[1][i] = ((m0 / 8 + 4 * lw + i) * (DFF / 8) + l3) * 128 + l7 * 16;
      vb[1][i] = (16 * lw + 4 * i + g) * 1024 + n0 * 2 + c * 16;
      va[2][i] = ((m0 / 8 + 8 * ki + 4 * (lw & 1) + i) * (DFF / 8) + 8 * (lw >> 1) + l3) * 128 + l7 * 16;
      vb[2][i] = (64 * ((c >> 3) ^ kj ^ ki) + 16 * lw + 4 * i + g) * 1024 + (n0 + 64 * kj) * 2 + (c & 7) * 16;
    }
#pragma unroll
    for (int i = 0; i < 4; ++i) asm volatile("" : "+v"(va[1][i]), "+v"(vb[1][i]), "+v"(va[2][i]), "+v"(vb[2][i]));
#pragma unroll 1
    for (int t = 0; t < 13; ++t) {
      WAIT_V(8);
      BAR();
      stage0();
    }
    STAMP(4);
    run(NA, va[1], vb[1], (const char*)h + 1024 * NA * ksl, (const char*)w2 + (size_t)65536 * NA * ksl, 1024, 65536);
    STAMP(5);
    {
      const int kb = ROT3 ? (2 * nt + (idx >> 2)) % NB : 0;
      run(NB - kb, va[2], vb[2], (const char*)h + KB0 * 16 + kb * 2048, (const char*)w2 + (size_t)KB0 * 1024 + kb * 131072, 2048, 131072);
      run(kb, va[2], vb[2], (const char*)h + KB0 * 16, (const char*)w2 + (size_t)KB0 * 1024, 2048, 131072);
    }
    STAMP(6);
    WAIT_V(8);
    BAR();
    WAIT_V(0);
    BAR();
    BAR();
    STAMP(8);
    STAMP_FLUSH(1);
  }
}

extern "C" void kernel_launch(void* const* d_in, const int* in_sizes, int n_in, void* d_out, int out_size,
                              void* d_ws, size_t ws_size, hipStream_t stream) {
  const u16* x = (const u16*)d_in[0];
  const u16* wsre = (const u16*)d_in[1];
  const u16* wsim = (const u16*)d_in[2];
  const u16* wdre = (const u16*)d_in[3];
  const u16* wdim = (const u16*)d_in[4];
  const u16* w1 = (const u16*)d_in[6];
  const u16* w2 = (const u16*)d_in[7];
  char* ws = (char*)d_ws;
  unsigned* cnt = (unsigned*)(ws + WS_CNT);
  u16* ab = (u16*)(ws + WS_AB);
  u16* h = (u16*)(ws + WS_H);
  k1_ln_gemm1<<<256, 512, 0, stream>>>(x, wdre, wdim, w1, ab, h, cnt, wsre, wsim, w2, (const u16*)d_out);
  k2_gemm2<<<256, 512, 0, stream>>>(wsre, wsim, h, ab, w2, x, (u16*)d_out, ws + WS_P, cnt);
}
```

```cpp
#include <hip/hip_runtime.h>
#include <hip/hip_bf16.h>
#include <stdint.h>

typedef __attribute__((ext_vector_type(8))) short bf16x8;
typedef __attribute__((ext_vector_type(4))) short s16x4;
typedef __attribute__((ext_vector_type(2))) short s16x2;
typedef __attribute__((ext_vector_type(4))) float f32x4;
typedef __attribute__((ext_vector_type(2))) float f32x2;
typedef __attribute__((ext_vector_type(2))) __bf16 bf16x2_t;
typedef __attribute__((ext_vector_type(4))) unsigned u32x4;
typedef __attribute__((ext_vector_type(2))) unsigned u32x2;
typedef __attribute__((ext_vector_type(4))) unsigned long long u64x4;
typedef unsigned short u16;

#define LDS_AS __attribute__((address_space(3)))
#define GLB_AS __attribute__((address_space(1)))
#define WAIT_V(n) asm volatile("s_waitcnt vmcnt(%0)" ::"n"(n) : "memory")
#define WAIT_L0() asm volatile("s_waitcnt lgkmcnt(0)" ::: "memory")
#define WAIT_L(n) __builtin_amdgcn_s_waitcnt(0xC07F | ((n) << 8))
#define BAR() do { asm volatile("" ::: "memory"); __builtin_amdgcn_s_barrier(); asm volatile("" ::: "memory"); } while (0)
#define SCHED() __builtin_amdgcn_sched_barrier(0)

#define STAMP_DECL
#define STAMP(i)
#define RSTAMP(i)
#define STAMP_FLUSH(k)

#define BLK_LAYOUT 1
constexpr int S_ = 2048, D_ = 512, DFF = 1792;
constexpr int HLD = DFF;

constexpr size_t WS_CNT = 0;
constexpr size_t WS_AB = 65536;
constexpr size_t WS_H = WS_AB + (size_t)4096 * 512 * 2;
constexpr size_t WS_P = WS_H + (size_t)2048 * 2048 * 2;

#define TSL 7, 6
#define NA 5
#define SIG_T 1
#define POLL_T 3
#define POLL_D 1
#define XL_T (POLL_T - 2)
#define ROT1 0
#define ROT2 2
#define ROT3 1
#define CPF1 1
#define CPF2 1
#define TPAT 2
#define ST0 (tl.skip ? 6 : 2 + (idx + (idx >> 2)) % 4)
#define ST1 7
#define K1CL 160
#define EARLY 1
#define EARLY2 1
#define EARLY_N 16
#define BALC 1
#define PRE 1
#define ZSK 1
#define MIX 1
#define PGT 1
#define TPOL ""
#define TOUCH64(d, p) asm volatile("global_load_dword %0, %1, off" TPOL : "+v"(d) : "v"(p) : "memory")
#define TOUCH(d, off, base) asm volatile("s_nop 4\n\tglobal_load_dword %0, %1, %2" : "+v"(d) : "v"(off), "s"(base) : "memory")

__device__ __forceinline__ void wait_v(int n) {
  switch (n) {
#define WV(k) case k: WAIT_V(k); break;
    WV(0) WV(1) WV(2) WV(3) WV(4) WV(5) WV(6) WV(10) WV(11) WV(12) WV(13) WV(14) WV(15) WV(16)
#undef WV
    default: WAIT_V(0);
  }
}
__device__ __forceinline__ float bf16_round(float f) { return __bfloat162float(__float2bfloat16(f)); }
__device__ __forceinline__ unsigned pack_bf16(float lo, float hi) {
  f32x2 v = {lo, hi};
  return __builtin_bit_cast(unsigned, __builtin_convertvector(v, bf16x2_t));
}
__device__ __forceinline__ float bf_lo(unsigned u) { return __builtin_bit_cast(float, u << 16); }
__device__ __forceinline__ float bf_hi(unsigned u) { return __builtin_bit_cast(float, u & 0xffff0000u); }
__device__ __forceinline__ __amdgpu_buffer_rsrc_t mkrsrc(const void* p) {
  return __builtin_amdgcn_make_buffer_rsrc((void*)p, 0, 0x7fffffff, 0x00020000);
}
__device__ __forceinline__ void blds16(__amdgpu_buffer_rsrc_t r, unsigned voff, char* l) {
  __builtin_amdgcn_raw_ptr_buffer_load_lds(r, (LDS_AS void*)l, 16, voff, 0, 0, 0);
}
__device__ __forceinline__ bf16x8 tr_read2(const char* p0, const char* p1) {
  s16x4 t0 = __builtin_amdgcn_ds_read_tr16_b64_v4i16((LDS_AS s16x4*)(p0));
  s16x4 t1 = __builtin_amdgcn_ds_read_tr16_b64_v4i16((LDS_AS s16x4*)(p1));
  return __builtin_shufflevector(t0, t1, 0, 1, 2, 3, 4, 5, 6, 7);
}
#define MFMA16(a, b, c) __builtin_amdgcn_mfma_f32_16x16x32_bf16(a, b, c, 0, 0, 0)
__device__ __forceinline__ bf16x8 split3(float c, bool z) {
  const float hi = bf16_round(c), r1 = c - hi, mid = bf16_round(r1), lo = r1 - mid;
  u32x4 v = {pack_bf16(hi, mid), pack_bf16(lo, 0.f), 0u, 0u};
  if (z) v = u32x4{0u, 0u, 0u, 0u};
  return __builtin_bit_cast(bf16x8, v);
}

constexpr int K1_W = 65536, K1_WS = 24576;
constexpr int K1_CODE_LINES = K1CL, K2_CODE_LINES = 84;
constexpr int KB0 = 4 * NA * 64;
constexpr int NB = (DFF - KB0) / 128;

struct Tile { bool nc, im, skip; int col0; };
__device__ __forceinline__ Tile tile_of(int bid) {
  const int ntile = 4 * ((bid & 7) >> 1) + ((bid >> 3) & 3);
  const unsigned long long T0 = MIX == 1 ? 0x704e838062400300ull : MIX == 2 ? 0x70834e8062034000ull : 0x4540a58380250300ull, T1 = MIX == 1 ? 0x595653a54b484525ull : MIX == 2 ? 0x5956a5534b482545ull : 0x595653704e4b4862ull;
  const unsigned e = (unsigned)((ntile < 8 ? T0 : T1) >> (8 * (ntile & 7)));
  return Tile{(e & 64) != 0, (e & 128) != 0, (e & 32) != 0, (int)(e & 31) * 64};
}

__device__ __forceinline__ void k1_loader(char* smem, int lw, u64x4 ka, int m0,
                                          const u16* pfa, const u16* pfb, const u16* pfc, const char* code2,
                                          const char* wsb, const char* outb, unsigned kd1, unsigned pfd, unsigned cf1) {
  const int tid = threadIdx.x, lane = tid & 63;
  STAMP_DECL
  RSTAMP(14);
  STAMP(0);
  const int xcd = blockIdx.x & 7, idx = blockIdx.x >> 3;
  unsigned vx[4], vw[6];
#pragma unroll
  for (int i = 0; i < 4; ++i)
    vx[i] = ((m0 + 32 * lw + 8 * i + (lane >> 3)) * (D_ * 2) + (((lane & 7) ^ ((lane >> 4) & 3)) * 16)) ^ ((i & 1) << 6);
  const char *x, *kp1, *kp2, *kp3;
  const char* wb;
  int wstep;
  auto stage_x = [&](int t) {
    const __amdgpu_buffer_rsrc_t px = mkrsrc((const char*)x + t * 128);
#pragma unroll
    for (int i = 0; i < 4; ++i) blds16(px, vx[i], smem + (t & 3) * 16384 + (4 * lw + i) * 1024);
  };
  auto stage_w = [&](int t) {
    const __amdgpu_buffer_rsrc_t pw = mkrsrc(wb + t * wstep);
#pragma unroll
    for (int i = 0; i < 6; ++i) blds16(pw, vw[i], smem + K1_W + (t & 3) * K1_WS + (6 * lw + i) * 1024);
  };
  SCHED();
  int b2 = blockIdx.x;
  asm volatile("" : "+s"(b2));
  const Tile tl = tile_of(b2);
  const bool nc = tl.nc;
  wstep = nc ? 64 * (DFF * 2) : 128;
  __builtin_amdgcn_s_setprio(3);
  if (nc) {
#pragma unroll
    for (int i = 0; i < 3; ++i) {
      const int id = 64 * i + lane, kr = id / 24, c = id % 24;
      vw[i] = (16 * lw + kr) * (DFF * 2) + ((c ^ (((kr >> 1) & 1) << 2)) * 16);
      if (ZSK && tl.skip && c < 8) vw[i] = 0x80000000u;
      vw[i + 3] = (vw[i] ^ 32) + 8 * (DFF * 2);
    }
  } else {
#pragma unroll
    for (int i = 0; i < 6; ++i)
      vw[i] = (((48 * lw + 8 * i + (lane >> 3)) * (D_ * 2) + (((lane & 7) ^ ((lane >> 4) & 3)) * 16)) ^ ((i & 1) << 6)) |
              (ZSK && tl.skip && 48 * lw + 8 * i < 64 ? 0x80000000u : 0u);
  }
#pragma unroll
  for (int i = 0; i < 6; ++i) asm volatile("" : "+v"(vw[i]));
  int wo = nc ? tl.col0 * 2 : tl.col0 * (D_ * 2);
  asm volatile("" : "+s"(wo));
  asm volatile("" : "+v"(vx[0]), "+v"(vx[1]), "+v"(vx[2]), "+v"(vx[3]));
  asm volatile("s_waitcnt lgkmcnt(0)\n\ts_mov_b64 %0, %4\n\ts_mov_b64 %1, %5\n\ts_mov_b64 %2, %6\n\ts_mov_b64 %3, %7"
               : "=&s"(x), "=&s"(kp1), "=&s"(kp2), "=&s"(kp3) : "s"(ka[0]), "s"(ka[1]), "s"(ka[2]), "s"(ka[3]), "s"(kd1));
  stage_x(0);
  wb = (nc ? kp3 : tl.im ? kp2 : kp1) + wo;
  stage_w(0);
  stage_x(1);
  stage_w(1);
  SCHED();
  const char* ta[4];
  {
    const int ksl = xcd >> 1, t2 = tid - 256;
    constexpr int LA = 2 * NA * 8, LB = (DFF - KB0) / 32 * 4;
    const int c = (2 * (idx >> 2) + 4 * (t2 & 1) + ((t2 >> 1) & 1)) & 7;
    ta[0] = (const char*)((t2 & 4) ? pfb : pfa) + (1024 * (xcd & 1) + 32 * idx + (t2 >> 3)) * 4096 + 1024 * ksl + c * 128;
    ta[1] = (const char*)((t2 & 4) ? pfb : pfa) + (1024 * (xcd & 1) + 32 * idx + (t2 >> 3)) * 4096 + 1024 * ksl + (c ^ 2) * 128;
    ta[0] = (const char*)((t2 & 128) ? pfb : pfa) + (1024 * (xcd & 1) + 32 * idx + ((t2 >> 3) & 15)) * 4096 + 1024 * ksl + (t2 & 7) * 128;
    ta[1] = ta[0] + 16 * 4096;
    const int tb = min(max(t2 - LA, 0), LB - 1);
    ta[2] = (const char*)pfc + (t2 < LA ? (64 * NA * ksl + 2 * NA * idx + (t2 >> 3)) * 1024 + (t2 & 7) * 128
                                        : (KB0 + (LB / 4) * idx + (tb >> 2)) * 1024 + (tb & 3) * 256 + (ksl & 1) * 128);
    const int j = min(11 * lw + min(lane, 10), 43);
    ta[3] = j < 37 ? wsb + 32768 + j * 524288 : j < 41 ? outb + (j - 37) * 524288
                   : code2 + min(3 * idx + j - 41, K2_CODE_LINES - 1) * 128;
  }
  constexpr int TP[2] = {TSL};
  const int tp0 = ST0, tp1 = ST1;
  auto ntouch = [&](int sg) { return (TP[0] == sg) + (TP[1] == sg); };
  asm volatile("" : "+v"(ta[0]), "+v"(ta[1]), "+v"(ta[2]), "+v"(ta[3]));
  STAMP(1);
  WAIT_V(10);
  asm volatile("" ::"v"(cf1));
  BAR();
  STAMP(2);
  stage_x(2);
  stage_w(2);
  if (tp0 == 2) TOUCH64(pfd, ta[0]);
#pragma unroll
  for (int t = 0; t < 8; ++t) {
    if (t < 7) {
      const int n = t + 1 < 7 ? ntouch(t + 1) + 10 + ntouch(t + 2) : ntouch(7);
      const int d = (tp0 == t + 1) + (tp0 == t + 2) + (tp1 == t + 1) + (tp1 == t + 2);
      if (d == 0) wait_v(n);
      else if (d == 1) wait_v(n + 1);
      else wait_v(n + 2);
    }
    BAR();
    if (t + 3 < 8) {
      stage_x(t + 3);
      stage_w(t + 3);
      if (TP[1] == t + 3) TOUCH64(pfd, ta[3]);
      if (tp0 == t + 3) TOUCH64(pfd, ta[0]);
      if (tp1 == t + 3) TOUCH64(pfd, ta[1]);
      if (TP[0] == t + 3) TOUCH64(pfd, ta[2]);
    }
  }
  STAMP(3);
  BAR();
  WAIT_V(0);
  asm volatile("" ::"v"(pfd));
  STAMP(12);
  STAMP_FLUSH(0);
}

template <bool NC>
__device__ __forceinline__ void k1_body(char* smem, int wid, u16* __restrict__ dst, int m0, int col0, bool skip64, float sgn,
                                        u64x4 ka, unsigned kd1, unsigned cf0, unsigned cf1) {
  const int tid = threadIdx.x, lane = tid & 63;
  STAMP_DECL
  RSTAMP(14);
  STAMP(0);
  float* stat = (float*)smem;
  const int wr = wid >> 1, wc = wid & 1;
  const int r16 = lane & 15, g = lane >> 4;
  int aoff[2][2], woff[2][4];
#pragma unroll
  for (int s = 0; s < 2; ++s)
#pragma unroll
    for (int e = 0; e < 2; ++e)
      aoff[s][e] = (64 * wr + 16 * (e ^ wc) + r16) * 128 + (((4 * s + g) ^ (r16 >> 1)) * 16);
#pragma unroll
  for (int j = 0; j < 4; ++j) {
    if constexpr (NC) {
      const int q = r16 >> 2, p = lane & 3;
      woff[0][j] = K1_W + (8 * g + q) * 384 + (((6 * wc + (j ^ wr)) ^ (q & 2) ^ (g & 1)) * 32) + p * 8;
    } else {
      woff[0][j] = K1_W + (96 * wc + 16 * ((j & 1) ^ wr) + r16) * 128 + (((4 * (j >> 1) + g) ^ (r16 >> 1)) * 16);
    }
    woff[1][j] = woff[0][j] + 2 * K1_WS;
    asm volatile("" : "+v"(woff[1][j]));
  }
  const f32x4 z4 = {0.f, 0.f, 0.f, 0.f};
  f32x4 acc[4][6], s1[2] = {z4, z4}, s2[2] = {z4, z4}, cs[3] = {z4, z4, z4};
#pragma unroll
  for (int v = 0; v < 6; ++v)
#pragma unroll
    for (int u = 0; u < 4; ++u) acc[u][v] = z4;
  bf16x8 ones;
#pragma unroll
  for (int e = 0; e < 8; ++e) ones[e] = (short)0x3F80;
  bf16x8 xa[2][4];
  s16x4 wf[2][6][2];
  auto rdn = [&](int set, int b, int n) {
    constexpr int TX[16] = {-1, -1, 0, 1, -1, -1, 2, -1, -1, 3, -1, -1, -1, -1, -1, -1};
    constexpr int TW[16] = {0, 1, -1, -1, 2, 3, -1, 4, 5, -1, 6, 7, 8, 9, 10, 11};
    if (TX[n] >= 0) {
      const int u = TX[n];
      xa[set][u] = *(const bf16x8*)(smem + aoff[set][u & 1] + (u >> 1) * 4096 + b * 16384);
    } else if constexpr (NC) {
      const int v = TW[n] >> 1, hi = TW[n] & 1;
      wf[set][v][hi] = __builtin_amdgcn_ds_read_tr16_b64_v4i16(
          (LDS_AS s16x4*)(smem + woff[b >> 1][v & 3] + (b & 1) * K1_WS + set * 12288 + hi * 1536 + (v >> 2) * 128));
    } else if (!(TW[n] & 1)) {
      const int v = TW[n] >> 1;
      const bf16x8 t = *(const bf16x8*)(smem + woff[b >> 1][2 * set + (v & 1)] + (b & 1) * K1_WS + (v >> 1) * 4096);
      wf[set][v][0] = __builtin_shufflevector(t, t, 0, 1, 2, 3);
      wf[set][v][1] = __builtin_shufflevector(t, t, 4, 5, 6, 7);
    }
  };
#define WF(set, v) __builtin_shufflevector(wf[set][v][0], wf[set][v][1], 0, 1, 2, 3, 4, 5, 6, 7)
  auto half = [&](int set, int b) {
    constexpr int OV[24] = {0, 0, 1, 1, 0, 1, 2, 2, 2, 0, 1, 2, 3, 3, 3, 3, 4, 4, 4, 4, 5, 5, 5, 5};
    constexpr int OU[24] = {0, 1, 0, 1, 2, 2, 0, 1, 2, 3, 3, 3, 0, 1, 2, 3, 0, 1, 2, 3, 0, 1, 2, 3};
    WAIT_L(NC ? 6 : 3);
#pragma unroll
    for (int n = 0; n < 24; ++n) {
      acc[OU[n]][OV[n]] = MFMA16(WF(set, OV[n]), xa[set][OU[n]], acc[OU[n]][OV[n]]);
      SCHED();
      if (n < 16) rdn(set ^ 1, b, n);
      if (n == 2 || n == 8) s1[n / 6] = MFMA16(ones, xa[set][2 * (n / 6)], s1[n / 6]);
      if (n == 5 || n == 11) s2[n / 6] = MFMA16(xa[set][2 * (n / 6)], xa[set][2 * (n / 6)], s2[n / 6]);
      if (n == 14 || n == 17 || n == 20) cs[(n - 14) / 3] = MFMA16(ones, WF(set, 2 * ((n - 14) / 3)), cs[(n - 14) / 3]);
      SCHED();
    }
  };
#pragma unroll
  for (int v = 0; v < 6; ++v) asm volatile("" : "+v"(acc[0][v]), "+v"(acc[1][v]), "+v"(acc[2][v]), "+v"(acc[3][v]));
  asm volatile("" : "+v"(s1[0]), "+v"(s1[1]), "+v"(s2[0]), "+v"(s2[1]), "+v"(cs[0]), "+v"(cs[1]), "+v"(cs[2]), "+v"(ones));
  asm volatile("s_waitcnt vmcnt(0) lgkmcnt(0)" ::"s"(ka), "s"(kd1), "v"(cf0), "v"(cf1));
  BAR();
  STAMP(2);
#pragma unroll
  for (int n = 0; n < 16; ++n) rdn(0, 0, n);
#pragma unroll 1
  for (int it = 0; it < 2; ++it) {
#pragma unroll
    for (int t = 0; t < 4; ++t) {
      SCHED();
      half(0, t);
      BAR();
      SCHED();
      half(1, (t + 1) & 3);
    }
  }
  STAMP(3);
  if (g == 0) {
#pragma unroll
    for (int w = 0; w < 3; ++w) stat[256 + 96 * wc + 16 * (2 * w + wr) + r16] = cs[w][0];
  }
  if (g == (r16 >> 2)) {
#pragma unroll
    for (int rg = 0; rg < 2; ++rg) {
      const float d = (lane & 2) ? ((lane & 1) ? s2[rg][3] : s2[rg][2]) : ((lane & 1) ? s2[rg][1] : s2[rg][0]);
      const float mean = s1[rg][0] * (1.f / 512.f);
      const float var = fmaxf(d * (1.f / 512.f) - mean * mean, 0.f);
      const float den = bf16_round(__builtin_amdgcn_sqrtf(bf16_round(bf16_round(var) + 1e-5f)));
      *(f32x2*)(stat + 2 * (64 * wr + 16 * (2 * rg + wc) + r16)) = f32x2{-mean, sgn * __builtin_amdgcn_rcpf(den)};
    }
  }
  WAIT_L0();
  BAR();
  STAMP(4);
  f32x2 st[4];
  float co[3];
#pragma unroll
  for (int u = 0; u < 4; ++u) st[u] = *(const f32x2*)(stat + 2 * (64 * wr + 16 * (u ^ wc) + r16));
#pragma unroll
  for (int w = 0; w < 3; ++w) co[w] = stat[256 + 96 * wc + 16 * (2 * w + (wr ^ 1)) + r16];
  bf16x8 xm[4];
#pragma unroll
  for (int u = 0; u < 4; ++u) {
    const unsigned nm = pack_bf16(st[u][0], st[u][0]);
    xm[u] = __builtin_bit_cast(bf16x8, u32x4{nm, nm & 0xffffu, 0u, 0u});
  }
  constexpr int NCH = NC ? DFF / 8 : D_ / 8;
  const __amdgpu_buffer_rsrc_t ro = mkrsrc((const char*)dst + ((size_t)(m0 >> 3) * NCH + col0 / 8) * 128);
  unsigned vo[4];
#pragma unroll
  for (int u = 0; u < 4; ++u)
    vo[u] = ((8 * wr + 2 * (u ^ wc) + (r16 >> 3)) * NCH + 12 * wc + 2 * ((g & 1) ^ wr) + (g >> 1)) * 128 + (r16 & 7) * 16;
#pragma unroll
  for (int w = 0; w < 3; ++w) {
    if (skip64 && wc == 0 && w < 2) continue;
#pragma unroll
    for (int s = 0; s < 2; ++s) {
      const bf16x8 wm = split3(s ? co[w] : cs[w][0], g != 0);
#pragma unroll
      for (int u = 0; u < 4; ++u) acc[u][2 * w + s] = MFMA16(wm, xm[u], acc[u][2 * w + s]);
    }
#pragma unroll
    for (int u = 0; u < 4; ++u) {
      unsigned pk[2][2];
#pragma unroll
      for (int s = 0; s < 2; ++s)
#pragma unroll
        for (int e = 0; e < 2; ++e) {
          unsigned t = pack_bf16(acc[u][2 * w + s][2 * e] * st[u][1], acc[u][2 * w + s][2 * e + 1] * st[u][1]);
          if constexpr (NC) t = __builtin_bit_cast(unsigned, __builtin_elementwise_max(__builtin_bit_cast(s16x2, t), s16x2{0, 0}));
          pk[s][e] = t;
        }
      const auto r0 = __builtin_amdgcn_permlane16_swap(pk[0][0], pk[1][0], false, false);
      const auto r1 = __builtin_amdgcn_permlane16_swap(pk[0][1], pk[1][1], false, false);
      __builtin_amdgcn_raw_buffer_store_b128(u32x4{r0[0], r1[0], r0[1], r1[1]}, ro, vo[u] + w * 512, 0, 16);
    }
  }
  STAMP(6);
  STAMP(7);
  STAMP_FLUSH(0);
}

__global__ __launch_bounds__(512, 2) void k1_ln_gemm1(const u16* __restrict__ x, const u16* __restrict__ wdre,
                                                      const u16* __restrict__ wdim, const u16* __restrict__ w1,
                                                      u16* __restrict__ ab, u16* __restrict__ h,
                                                      unsigned* __restrict__ cnt, const u16* __restrict__ wsre,
                                                      const u16* __restrict__ wsim, const u16* __restrict__ w2,
                                                      const u16* outp) {
  __shared__ __attribute__((aligned(1024))) char smem[K1_W + 4 * K1_WS];
  const char* code1;
  int cdist;
  u64x4 ka;
  unsigned kd1, cf0, cf1;
  asm volatile("s_getpc_b64 %0\nk1_code_mark:\n\ts_load_dwordx8 %2, %4, 0x0\n\ts_load_dword %3, %4, 0x40\n\ts_mov_b32 %1, k2_code_mark-k1_code_mark"
               : "=&s"(code1), "=&s"(cdist), "=&s"(ka), "=&s"(kd1) : "s"(__builtin_amdgcn_kernarg_segment_ptr()));
  asm volatile("v_and_b32 %0, 15, %2\n\tv_lshlrev_b32 %0, 7, %0\n\tglobal_load_dword %0, %0, %3\n\t"
               "v_and_b32 %1, 7, %2\n\tv_add_lshl_u32 %1, %1, %4, 7\n\tglobal_load_dword %1, %1, %3 offset:2048"
               : "=&v"(cf0), "=&v"(cf1) : "v"(threadIdx.x), "s"(code1), "s"(blockIdx.x & 0x78) : "memory");
  SCHED();
  const int wid = __builtin_amdgcn_readfirstlane(threadIdx.x >> 6);
  const int m0 = (8 * (blockIdx.x & 1) + (blockIdx.x >> 5)) * 128;
  if (wid < 4) {
    const Tile tl = tile_of(blockIdx.x);
    if (threadIdx.x == 0) cnt[blockIdx.x * 16] = 0;
    if (!tl.nc) k1_body<false>(smem, wid, ab + (size_t)tl.im * 2048 * D_, m0, tl.col0, tl.skip, tl.im ? -1.f : 1.f, ka, kd1, cf0, cf1);
    else k1_body<true>(smem, wid, h, m0, tl.col0, tl.skip, 1.f, ka, kd1, cf0, cf1);
    return;
  }
  k1_loader(smem, wid - 4, ka, m0, wsre, wsim, w2, code1 + cdist, (const char*)cnt, (const char*)outp, kd1, cf0, cf1);
}

constexpr int K2_NA = 16 + NA, K2_NT = K2_NA + NB, K2_B = 65536;

__global__ __launch_bounds__(512, 2) void k2_gemm2(const u16* __restrict__ wsre_, const u16* __restrict__ wsim_,
                                                   const u16* __restrict__ h_, const u16* __restrict__ ab_,
                                                   const u16* __restrict__ w2_, const u16* __restrict__ x,
                                                   u16* __restrict__ out, char* __restrict__ part,
                                                   unsigned* __restrict__ cnt) {
  __shared__ __attribute__((aligned(1024))) char smem[2 * K2_B];
  u64x4 ka;
  unsigned long long kw;
  unsigned kd1;
  asm volatile("k2_code_mark:\n\ts_load_dwordx8 %0, %3, 0x0\n\ts_load_dwordx2 %1, %3, 0x20\n\ts_load_dword %2, %3, 0x40"
               : "=&s"(ka), "=&s"(kw), "=&s"(kd1) : "s"(__builtin_amdgcn_kernarg_segment_ptr()));
  SCHED();
  const int tid = threadIdx.x, lane = tid & 63;
  const int wid = __builtin_amdgcn_readfirstlane(tid >> 6);
  const int bid = blockIdx.x, xcd = bid & 7, idx = bid >> 3;
  const int ksl = xcd >> 1, mt = 8 * (xcd & 1) + (idx >> 2), nt = idx & 3;
  const int ki = ksl >> 1, kj = ksl & 1;
  const int m0 = mt * 128, n0 = nt * 128;
  const int r16 = lane & 15, g = lane >> 4;
  STAMP_DECL
  RSTAMP(14);
  STAMP(0);
  if (wid < 4) {
  const int wr = wid >> 1, wc = wid & 1;
  const int q4 = r16 >> 2, p4 = lane & 3;
  int aoff[2][2], boff[4][2];
#pragma unroll
  for (int s = 0; s < 2; ++s)
#pragma unroll
    for (int i = 0; i < 2; ++i) aoff[s][i] = (64 * (i ^ ki) + 32 * wr + r16) * 128 + (((4 * s + g) ^ (r16 >> 1)) * 16);
#pragma unroll
  for (int f = 0; f < 4; ++f) {
    boff[f][0] = K2_B + g * 2048 + (8 * ((f >> 1) ^ kj) + 4 * wc + 2 * (f & 1) + (p4 >> 1)) * 128 + (q4 + 4 * (g & 1)) * 16 + 8 * (p4 & 1);
    boff[f][1] = boff[f][0] ^ 64;
  }
  const f32x4 z4 = {0.f, 0.f, 0.f, 0.f};
  f32x4 acc[4][4];
#pragma unroll
  for (int a = 0; a < 4; ++a)
#pragma unroll
    for (int b = 0; b < 4; ++b) acc[a][b] = z4;
  bf16x8 am[2][4];
  s16x4 bm[2][4][2];
  auto rdn = [&](int set, int b, int n) {
    constexpr int TA[12] = {-1, -1, 0, 1, -1, -1, 2, -1, -1, 3, -1, -1};
    constexpr int TB[12] = {0, 1, -1, -1, 2, 3, -1, 4, 5, -1, 6, 7};
    if (TA[n] >= 0) am[set][TA[n]] = *(const bf16x8*)(smem + aoff[set][TA[n] >> 1] + (TA[n] & 1) * 2048 + b * 16384);
    else
      bm[set][TB[n] >> 1][TB[n] & 1] = __builtin_amdgcn_ds_read_tr16_b64_v4i16(
          (LDS_AS s16x4*)(smem + boff[TB[n] >> 1][TB[n] & 1] + set * 8192 + b * 16384));
  };
#define BM(set, f) __builtin_shufflevector(bm[set][f][0], bm[set][f][1], 0, 1, 2, 3, 4, 5, 6, 7)
  auto half = [&](int set, int b) {
    constexpr int OB[16] = {0, 0, 1, 1, 0, 1, 2, 2, 2, 0, 1, 2, 3, 3, 3, 3};
    constexpr int OA[16] = {0, 1, 0, 1, 2, 2, 0, 1, 2, 3, 3, 3, 0, 1, 2, 3};
    WAIT_L(3);
#pragma unroll
    for (int n = 0; n < 16; ++n) {
      acc[OA[n]][OB[n]] = MFMA16(BM(set, OB[n]), am[set][OA[n]], acc[OA[n]][OB[n]]);
      SCHED();
      if (n < 12) rdn(set ^ 1, b, n);
      SCHED();
    }
  };
  auto halfb = [&](int set, int b) {
#pragma unroll
    for (int n = 0; n < 8; ++n) {
      const int i = n >> 2, bb = (n >> 1) & 1, a = n & 1;
      acc[a][bb] = MFMA16(BM(set, 2 * i + bb), am[set][2 * i + a], acc[a][bb]);
      SCHED();
      if (n < 6) { rdn(set ^ 1, b, 2 * n); rdn(set ^ 1, b, 2 * n + 1); }
      SCHED();
    }
  };
#pragma unroll
  for (int a = 0; a < 4; ++a) asm volatile("" : "+v"(acc[a][0]), "+v"(acc[a][1]), "+v"(acc[a][2]), "+v"(acc[a][3]));
  asm volatile("s_waitcnt lgkmcnt(0)" ::"s"(ka), "s"(kw), "s"(kd1));
  BAR();
  STAMP(2);
#pragma unroll
  for (int n = 0; n < 12; ++n) rdn(0, 0, n);
#pragma unroll 1
  for (int it = 0; it < 4; ++it) {
#pragma unroll
    for (int t = 0; t < 4; ++t) {
      SCHED();
      half(0, t);
      BAR();
      if (t == 3 && it == 3) {
#pragma unroll
        for (int s = 0; s < 2; ++s)
#pragma unroll
          for (int i = 0; i < 2; ++i) aoff[s][i] = (8 * (i ^ ki) + 4 * wr + (r16 >> 3)) * 1024 + (4 * s + g) * 128 + (r16 & 7) * 16;
#pragma unroll
        for (int f = 0; f < 4; ++f) {
          boff[f][0] = K2_B + (8 * g + q4) * 256 + (((8 * ((f >> 1) ^ kj) + 4 * wc + 2 * (f & 1) + (p4 >> 1)) ^ (q4 << 2) ^ ((g & 1) << 1)) * 16) + 8 * (p4 & 1);
          boff[f][1] = boff[f][0] + 1024;
        }
      }
      SCHED();
      half(1, (t + 1) & 3);
    }
  }
#pragma unroll
  for (int t = 16; t < K2_NA; ++t) {
    SCHED();
    half(0, t & 3);
    BAR();
    SCHED();
    half(1, (t + 1) & 3);
  }
  STAMP(8);
  const int tile = mt * 4 + nt;
  const int voff = wid * 2048 + lane * 16;
#pragma unroll
  for (int s = 1; s < 4; ++s) {
    const __amdgpu_buffer_rsrc_t rp = mkrsrc(part + ((tile * 4 + (ksl ^ s)) * 3 + (s - 1)) * 8192);
#pragma unroll
    for (int a = 0; a < 2; ++a) {
      const f32x4 c0 = acc[(s & 2) + a][2 * (s & 1)], c1 = acc[(s & 2) + a][2 * (s & 1) + 1];
      const u32x4 v = {pack_bf16(c0[0], c0[1]), pack_bf16(c0[2], c0[3]), pack_bf16(c1[0], c1[1]), pack_bf16(c1[2], c1[3])};
      __builtin_amdgcn_raw_buffer_store_b128(v, rp, voff + a * 1024, 0, 16);
    }
  }
  STAMP(9);
  const __amdgpu_buffer_rsrc_t rq = mkrsrc(part + (tile * 4 + ksl) * 3 * 8192);
  const size_t obase = (size_t)(m0 + 64 * ki + 32 * wr + r16) * D_ + n0 + 64 * kj + 32 * wc + 16 * (g & 1) + 8 * (g >> 1);
  GLB_AS unsigned* mine = (GLB_AS unsigned*)(cnt + (tile * 4 + ksl) * 16);
  u32x4 xv[2], pv[3][2];
  unsigned seen = 0;
#pragma unroll
  for (int t = 0; t < NB; ++t) {
    SCHED();
    halfb(0, (K2_NA + t) & 3);
    if (t == SIG_T) { STAMP(10); WAIT_V(0); STAMP(11); }
    if (t == POLL_T && wid == 0) {
      STAMP(4);
      for (int spin = 0; spin < (1 << 22) && __builtin_amdgcn_readfirstlane(seen) < 3u; ++spin) {
        __builtin_amdgcn_s_sleep(1);
        seen = __hip_atomic_load(mine, __ATOMIC_RELAXED, __HIP_MEMORY_SCOPE_AGENT);
      }
      STAMP(5);
    }
    BAR();
    if (t == SIG_T && wid == 0 && lane >= 1 && lane < 4)
      __hip_atomic_fetch_add((GLB_AS unsigned*)(cnt + (tile * 4 + (ksl ^ lane)) * 16), 1u, __ATOMIC_RELAXED, __HIP_MEMORY_SCOPE_AGENT);
    if (t == XL_T) {
#pragma unroll
      for (int a = 0; a < 2; ++a) xv[a] = *(const u32x4*)(x + obase + (size_t)16 * D_ * a);
    }
    if (t == POLL_T - POLL_D && wid == 0) seen = __hip_atomic_load(mine, __ATOMIC_RELAXED, __HIP_MEMORY_SCOPE_AGENT);
    if (t == POLL_T) {
#pragma unroll
      for (int s = 0; s < 3; ++s)
#pragma unroll
        for (int a = 0; a < 2; ++a)
          pv[s][a] = __builtin_bit_cast(u32x4, __builtin_amdgcn_raw_buffer_load_b128(rq, voff + a * 1024 + s * 8192, 0, 16));
    }
    SCHED();
    halfb(1, (K2_NA + t + 1) & 3);
  }
  STAMP(6);
#pragma unroll
  for (int a = 0; a < 2; ++a) {
    const auto x0 = __builtin_amdgcn_permlane16_swap(xv[a][0], xv[a][2], false, false);
    const auto x1 = __builtin_amdgcn_permlane16_swap(xv[a][1], xv[a][3], false, false);
    const unsigned xx[4] = {x0[0], x1[0], x0[1], x1[1]};
    unsigned o[4];
#pragma unroll
    for (int e = 0; e < 4; ++e) {
      const unsigned p0 = pv[0][a][e], p1 = pv[1][a][e], p2 = pv[2][a][e];
      const float lo = ((acc[a][e >> 1][2 * (e & 1)] + bf_lo(p0)) + (bf_lo(p1) + bf_lo(p2))) + bf_lo(xx[e]);
      const float hi = ((acc[a][e >> 1][2 * (e & 1) + 1] + bf_hi(p0)) + (bf_hi(p1) + bf_hi(p2))) + bf_hi(xx[e]);
      o[e] = pack_bf16(lo, hi);
    }
    const auto r0 = __builtin_amdgcn_permlane16_swap(o[0], o[2], false, false);
    const auto r1 = __builtin_amdgcn_permlane16_swap(o[1], o[3], false, false);
    *(u32x4*)(out + obase + (size_t)16 * D_ * a) = u32x4{r0[0], r1[0], r0[1], r1[1]};
  }
  STAMP(7);
  STAMP(12);
  STAMP_FLUSH(1);
  return;
  }
  {
    __builtin_amdgcn_s_setprio(3);
    const int lw = wid - 4;
    const int l3 = lane >> 3, l7 = lane & 7;
    unsigned va[3][4], vb[3][4];
#pragma unroll
    for (int i = 0; i < 4; ++i) {
      va[0][i] = ((m0 + 32 * lw + 8 * i + l3) * 4096 + ((l7 ^ (g & 3)) * 16)) ^ ((i & 1) << 6);
      vb[0][i] = ((2 * lw + (i >> 1)) * 64 + n0 / 8 + 8 * (i & 1) + l3) * 128 + ((l7 ^ (4 * (i >> 1))) * 16);
    }
#pragma unroll
    for (int i = 0; i < 4; ++i) asm volatile("" : "+v"(va[0][i]), "+v"(vb[0][i]));
    const char *wsre, *wsim, *h, *ab, *w2;
    asm volatile("s_waitcnt lgkmcnt(0)\n\ts_mov_b64 %0, %5\n\ts_mov_b64 %1, %6\n\ts_mov_b64 %2, %7\n\ts_mov_b64 %3, %8\n\ts_mov_b64 %4, %9"
                 : "=&s"(wsre), "=&s"(wsim), "=&s"(h), "=&s"(ab), "=&s"(w2) : "s"(ka[0]), "s"(ka[1]), "s"(ka[2]), "s"(ka[3]), "s"(kw), "s"(kd1));
    int lbuf = 0;
    auto stage = [&](const unsigned (&oa)[4], const unsigned (&ob)[4], const char* pa, const char* pb) {
      char* lb = smem + lbuf * 16384 + lw * 4096;
      lbuf = (lbuf + 1) & 3;
      const __amdgpu_buffer_rsrc_t ra = mkrsrc(pa), rb = mkrsrc(pb);
#pragma unroll
      for (int i = 0; i < 4; ++i) blds16(rb, ob[i], lb + K2_B + i * 1024);
#pragma unroll
      for (int i = 0; i < 4; ++i) blds16(ra, oa[i], lb + i * 1024);
    };
    auto run = [&](int n, const unsigned (&oa)[4], const unsigned (&ob)[4], const char* pa, const char* pb, int sa, int sb) {
#pragma unroll 1
      for (int t = 0; t < n; ++t, pa += sa, pb += sb) {
        WAIT_V(8);
        BAR();
        stage(oa, ob, pa, pb);
      }
    };
    int ks = ROT2 == 1 ? (4 * nt + 2 * (idx >> 2)) & 15 : ROT2 == 2 ? (8 * ((idx >> 2) & 1)) : ROT2 == 3 ? (4 * ((idx >> 2) & 3)) : 0;
    auto stage0 = [&]() {
      stage(va[0], vb[0], (const char*)((ks & 8) ? wsim : wsre) + 1024 * ksl + 128 * (ks & 7),
            (const char*)ab + ksl * 524288 + (ks >> 3) * 2097152 + (ks & 7) * 65536);
      ks = (ks + 1) & 15;
    };
    stage0();
    stage0();
    STAMP(1);
    WAIT_V(8);
    BAR();
    stage0();
    SCHED();
#pragma unroll
    for (int i = 0; i < 4; ++i) {
      const int c = r16 ^ (g << 2) ^ ((i >> 1) << 1);
      va[1][i] = ((m0 / 8 + 4 * lw + i) * (DFF / 8) + l3) * 128 + l7 * 16;
      vb[1][i] = (16 * lw + 4 * i + g) * 1024 + n0 * 2 + c * 16;
      va[2][i] = ((m0 / 8 + 8 * ki + 4 * (lw & 1) + i) * (DFF / 8) + 8 * (lw >> 1) + l3) * 128 + l7 * 16;
      vb[2][i] = (64 * ((c >> 3) ^ kj ^ ki) + 16 * lw + 4 * i + g) * 1024 + (n0 + 64 * kj) * 2 + (c & 7) * 16;
    }
#pragma unroll
    for (int i = 0; i < 4; ++i) asm volatile("" : "+v"(va[1][i]), "+v"(vb[1][i]), "+v"(va[2][i]), "+v"(vb[2][i]));
#pragma unroll 1
    for (int t = 0; t < 13; ++t) {
      WAIT_V(8);
      BAR();
      stage0();
    }
    STAMP(4);
    run(NA, va[1], vb[1], (const char*)h + 1024 * NA * ksl, (const char*)w2 + (size_t)65536 * NA * ksl, 1024, 65536);
    STAMP(5);
    {
      const int kb = ROT3 ? (2 * nt + (idx >> 2)) % NB : 0;
      run(NB - kb, va[2], vb[2], (const char*)h + KB0 * 16 + kb * 2048, (const char*)w2 + (size_t)KB0 * 1024 + kb * 131072, 2048, 131072);
      run(kb, va[2], vb[2], (const char*)h + KB0 * 16, (const char*)w2 + (size_t)KB0 * 1024, 2048, 131072);
    }
    STAMP(6);
    WAIT_V(8);
    BAR();
    WAIT_V(0);
    BAR();
    BAR();
    STAMP(8);
    STAMP_FLUSH(1);
  }
}

extern "C" void kernel_launch(void* const* d_in, const int* in_sizes, int n_in, void* d_out, int out_size,
                              void* d_ws, size_t ws_size, hipStream_t stream) {
  const u16* x = (const u16*)d_in[0];
  const u16* wsre = (const u16*)d_in[1];
  const u16* wsim = (const u16*)d_in[2];
  const u16* wdre = (const u16*)d_in[3];
  const u16* wdim = (const u16*)d_in[4];
  const u16* w1 = (const u16*)d_in[6];
  const u16* w2 = (const u16*)d_in[7];
  char* ws = (char*)d_ws;
  unsigned* cnt = (unsigned*)(ws + WS_CNT);
  u16* ab = (u16*)(ws + WS_AB);
  u16* h = (u16*)(ws + WS_H);
  k1_ln_gemm1<<<256, 512, 0, stream>>>(x, wdre, wdim, w1, ab, h, cnt, wsre, wsim, w2, (const u16*)d_out);
  k2_gemm2<<<256, 512, 0, stream>>>(wsre, wsim, h, ab, w2, x, (u16*)d_out, ws + WS_P, cnt);
}
```

```cpp
#include <hip/hip_runtime.h>
#include <hip/hip_bf16.h>
#include <stdint.h>

typedef __attribute__((ext_vector_type(8))) short bf16x8;
typedef __attribute__((ext_vector_type(4))) short s16x4;
typedef __attribute__((ext_vector_type(2))) short s16x2;
typedef __attribute__((ext_vector_type(4))) float f32x4;
typedef __attribute__((ext_vector_type(2))) float f32x2;
typedef __attribute__((ext_vector_type(2))) __bf16 bf16x2_t;
typedef __attribute__((ext_vector_type(4))) unsigned u32x4;
typedef __attribute__((ext_vector_type(2))) unsigned u32x2;
typedef __attribute__((ext_vector_type(4))) unsigned long long u64x4;
typedef unsigned short u16;

#define LDS_AS __attribute__((address_space(3)))
#define GLB_AS __attribute__((address_space(1)))
#define WAIT_V(n) asm volatile("s_waitcnt vmcnt(%0)" ::"n"(n) : "memory")
#define WAIT_L0() asm volatile("s_waitcnt lgkmcnt(0)" ::: "memory")
#define WAIT_L(n) __builtin_amdgcn_s_waitcnt(0xC07F | ((n) << 8))
#define BAR() do { asm volatile("" ::: "memory"); __builtin_amdgcn_s_barrier(); asm volatile("" ::: "memory"); } while (0)
#define SCHED() __builtin_amdgcn_sched_barrier(0)

#define STAMP_DECL
#define STAMP(i)
#define RSTAMP(i)
#define STAMP_FLUSH(k)

#define BLK_LAYOUT 1
constexpr int S_ = 2048, D_ = 512, DFF = 1792;
constexpr int HLD = DFF;

constexpr size_t WS_CNT = 0;
constexpr size_t WS_AB = 65536;
constexpr size_t WS_H = WS_AB + (size_t)4096 * 512 * 2;
constexpr size_t WS_P = WS_H + (size_t)2048 * 2048 * 2;

#define TSL 7, 6
#define NA 5
#define SIG_T 1
#define POLL_T 3
#define POLL_D 1
#define XL_T (POLL_T - 2)
#define ROT1 0
#define ROT2 2
#define ROT3 1
#define CPF1 1
#define CPF2 1
#define TPAT 2
#define ST0 (tl.skip ? 6 : 2 + (idx + (idx >> 2)) % 4)
#define ST1 7
#define K1CL 160
#define EARLY 1
#define EARLY2 1
#define EARLY_N 16
#define BALC 1
#define PRE 1
#define ZSK 1
#define MIX 1
#define PGE 1
#define PGT 1
#define TPOL ""
#define TOUCH64(d, p) asm volatile("global_load_dword %0, %1, off" TPOL : "+v"(d) : "v"(p) : "memory")
#define TOUCH(d, off, base) asm volatile("s_nop 4\n\tglobal_load_dword %0, %1, %2" : "+v"(d) : "v"(off), "s"(base) : "memory")

__device__ __forceinline__ void wait_v(int n) {
  switch (n) {
#define WV(k) case k: WAIT_V(k); break;
    WV(0) WV(1) WV(2) WV(3) WV(4) WV(5) WV(6) WV(10) WV(11) WV(12) WV(13) WV(14) WV(15) WV(16)
#undef WV
    default: WAIT_V(0);
  }
}
__device__ __forceinline__ float bf16_round(float f) { return __bfloat162float(__float2bfloat16(f)); }
__device__ __forceinline__ unsigned pack_bf16(float lo, float hi) {
  f32x2 v = {lo, hi};
  return __builtin_bit_cast(unsigned, __builtin_convertvector(v, bf16x2_t));
}
__device__ __forceinline__ float bf_lo(unsigned u) { return __builtin_bit_cast(float, u << 16); }
__device__ __forceinline__ float bf_hi(unsigned u) { return __builtin_bit_cast(float, u & 0xffff0000u); }
__device__ __forceinline__ __amdgpu_buffer_rsrc_t mkrsrc(const void* p) {
  return __builtin_amdgcn_make_buffer_rsrc((void*)p, 0, 0x7fffffff, 0x00020000);
}
__device__ __forceinline__ void blds16(__amdgpu_buffer_rsrc_t r, unsigned voff, char* l) {
  __builtin_amdgcn_raw_ptr_buffer_load_lds(r, (LDS_AS void*)l, 16, voff, 0, 0, 0);
}
__device__ __forceinline__ bf16x8 tr_read2(const char* p0, const char* p1) {
  s16x4 t0 = __builtin_amdgcn_ds_read_tr16_b64_v4i16((LDS_AS s16x4*)(p0));
  s16x4 t1 = __builtin_amdgcn_ds_read_tr16_b64_v4i16((LDS_AS s16x4*)(p1));
  return __builtin_shufflevector(t0, t1, 0, 1, 2, 3, 4, 5, 6, 7);
}
#define MFMA16(a, b, c) __builtin_amdgcn_mfma_f32_16x16x32_bf16(a, b, c, 0, 0, 0)
__device__ __forceinline__ bf16x8 split3(float c, bool z) {
  const float hi = bf16_round(c), r1 = c - hi, mid = bf16_round(r1), lo = r1 - mid;
  u32x4 v = {pack_bf16(hi, mid), pack_bf16(lo, 0.f), 0u, 0u};
  if (z) v = u32x4{0u, 0u, 0u, 0u};
  return __builtin_bit_cast(bf16x8, v);
}

constexpr int K1_W = 65536, K1_WS = 24576;
constexpr int K1_CODE_LINES = K1CL, K2_CODE_LINES = 84;
constexpr int KB0 = 4 * NA * 64;
constexpr int NB = (DFF - KB0) / 128;

struct Tile { bool nc, im, skip; int col0; };
__device__ __forceinline__ Tile tile_of(int bid) {
  const int ntile = 4 * ((bid & 7) >> 1) + ((bid >> 3) & 3);
  const unsigned long long T0 = MIX == 1 ? 0x704e838062400300ull : MIX == 2 ? 0x70834e8062034000ull : 0x4540a58380250300ull, T1 = MIX == 1 ? 0x595653a54b484525ull : MIX == 2 ? 0x5956a5534b482545ull : 0x595653704e4b4862ull;
  const unsigned e = (unsigned)((ntile < 8 ? T0 : T1) >> (8 * (ntile & 7)));
  return Tile{(e & 64) != 0, (e & 128) != 0, (e & 32) != 0, (int)(e & 31) * 64};
}

__device__ __forceinline__ void k1_loader(char* smem, int lw, u64x4 ka, int m0,
                                          const u16* pfa, const u16* pfb, const u16* pfc, const char* code2,
                                          const char* wsb, const char* outb, unsigned kd1, unsigned pfd, unsigned cf1) {
  const int tid = threadIdx.x, lane = tid & 63;
  STAMP_DECL
  RSTAMP(14);
  STAMP(0);
  const int xcd = blockIdx.x & 7, idx = blockIdx.x >> 3;
  unsigned vx[4], vw[6];
#pragma unroll
  for (int i = 0; i < 4; ++i)
    vx[i] = ((m0 + 32 * lw + 8 * i + (lane >> 3)) * (D_ * 2) + (((lane & 7) ^ ((lane >> 4) & 3)) * 16)) ^ ((i & 1) << 6);
  const char *x, *kp1, *kp2, *kp3;
  const char* wb;
  int wstep;
  auto stage_x = [&](int t) {
    const __amdgpu_buffer_rsrc_t px = mkrsrc((const char*)x + t * 128);
#pragma unroll
    for (int i = 0; i < 4; ++i) blds16(px, vx[i], smem + (t & 3) * 16384 + (4 * lw + i) * 1024);
  };
  auto stage_w = [&](int t) {
    const __amdgpu_buffer_rsrc_t pw = mkrsrc(wb + t * wstep);
#pragma unroll
    for (int i = 0; i < 6; ++i) blds16(pw, vw[i], smem + K1_W + (t & 3) * K1_WS + (6 * lw + i) * 1024);
  };
  SCHED();
  int b2 = blockIdx.x;
  asm volatile("" : "+s"(b2));
  const Tile tl = tile_of(b2);
  const bool nc = tl.nc;
  wstep = nc ? 64 * (DFF * 2) : 128;
  __builtin_amdgcn_s_setprio(3);
  if (nc) {
#pragma unroll
    for (int i = 0; i < 3; ++i) {
      const int id = 64 * i + lane, kr = id / 24, c = id % 24;
      vw[i] = (16 * lw + kr) * (DFF * 2) + ((c ^ (((kr >> 1) & 1) << 2)) * 16);
      if (ZSK && tl.skip && c < 8) vw[i] = 0x80000000u;
      vw[i + 3] = (vw[i] ^ 32) + 8 * (DFF * 2);
    }
  } else {
#pragma unroll
    for (int i = 0; i < 6; ++i)
      vw[i] = (((48 * lw + 8 * i + (lane >> 3)) * (D_ * 2) + (((lane & 7) ^ ((lane >> 4) & 3)) * 16)) ^ ((i & 1) << 6)) |
              (ZSK && tl.skip && 48 * lw + 8 * i < 64 ? 0x80000000u : 0u);
  }
#pragma unroll
  for (int i = 0; i < 6; ++i) asm volatile("" : "+v"(vw[i]));
  int wo = nc ? tl.col0 * 2 : tl.col0 * (D_ * 2);
  asm volatile("" : "+s"(wo));
  asm volatile("" : "+v"(vx[0]), "+v"(vx[1]), "+v"(vx[2]), "+v"(vx[3]));
  asm volatile("s_waitcnt lgkmcnt(0)\n\ts_mov_b64 %0, %4\n\ts_mov_b64 %1, %5\n\ts_mov_b64 %2, %6\n\ts_mov_b64 %3, %7"
               : "=&s"(x), "=&s"(kp1), "=&s"(kp2), "=&s"(kp3) : "s"(ka[0]), "s"(ka[1]), "s"(ka[2]), "s"(ka[3]), "s"(kd1));
  stage_x(0);
  wb = (nc ? kp3 : tl.im ? kp2 : kp1) + wo;
  stage_w(0);
  stage_x(1);
  stage_w(1);
  SCHED();
  const char* ta[4];
  {
    const int ksl = xcd >> 1, t2 = tid - 256;
    constexpr int LA = 2 * NA * 8, LB = (DFF - KB0) / 32 * 4;
    const int c = (2 * (idx >> 2) + 4 * (t2 & 1) + ((t2 >> 1) & 1)) & 7;
    ta[0] = (const char*)((t2 & 4) ? pfb : pfa) + (1024 * (xcd & 1) + 32 * idx + (t2 >> 3)) * 4096 + 1024 * ksl + c * 128;
    ta[1] = (const char*)((t2 & 4) ? pfb : pfa) + (1024 * (xcd & 1) + 32 * idx + (t2 >> 3)) * 4096 + 1024 * ksl + (c ^ 2) * 128;
    ta[0] = (const char*)((t2 & 128) ? pfb : pfa) + (1024 * (xcd & 1) + 32 * idx + ((t2 >> 3) & 15)) * 4096 + 1024 * ksl + (t2 & 7) * 128;
    ta[1] = ta[0] + 16 * 4096;
    const int tb = min(max(t2 - LA, 0), LB - 1);
    ta[2] = (const char*)pfc + (t2 < LA ? (64 * NA * ksl + 2 * NA * idx + (t2 >> 3)) * 1024 + (t2 & 7) * 128
                                        : (KB0 + (LB / 4) * idx + (tb >> 2)) * 1024 + (tb & 3) * 256 + (ksl & 1) * 128);
    const int j = min(11 * lw + min(lane, 10), 43);
    ta[3] = j < 37 ? wsb + 32768 + j * 524288 : j < 41 ? outb + (j - 37) * 524288
                   : code2 + min(3 * idx + j - 41, K2_CODE_LINES - 1) * 128;
  }
  constexpr int TP[2] = {TSL};
  const int tp0 = ST0, tp1 = ST1;
  auto ntouch = [&](int sg) { return (TP[0] == sg) + (TP[1] == sg); };
  asm volatile("" : "+v"(ta[0]), "+v"(ta[1]), "+v"(ta[2]), "+v"(ta[3]));
  STAMP(1);
  WAIT_V(10);
  asm volatile("" ::"v"(cf1));
  BAR();
  STAMP(2);
  stage_x(2);
  stage_w(2);
  if (tp0 == 2) TOUCH64(pfd, ta[0]);
#pragma unroll
  for (int t = 0; t < 8; ++t) {
    if (t < 7) {
      const int n = t + 1 < 7 ? ntouch(t + 1) + 10 + ntouch(t + 2) : ntouch(7);
      const int d = (tp0 == t + 1) + (tp0 == t + 2) + (tp1 == t + 1) + (tp1 == t + 2);
      if (d == 0) wait_v(n);
      else if (d == 1) wait_v(n + 1);
      else wait_v(n + 2);
    }
    BAR();
    if (t + 3 < 8) {
      stage_x(t + 3);
      stage_w(t + 3);
      if (TP[1] == t + 3) TOUCH64(pfd, ta[3]);
      if (tp0 == t + 3) TOUCH64(pfd, ta[0]);
      if (tp1 == t + 3) TOUCH64(pfd, ta[1]);
      if (TP[0] == t + 3) TOUCH64(pfd, ta[2]);
    }
  }
  STAMP(3);
  BAR();
  WAIT_V(0);
  asm volatile("" ::"v"(pfd));
  STAMP(12);
  STAMP_FLUSH(0);
}

template <bool NC>
__device__ __forceinline__ void k1_body(char* smem, int wid, u16* __restrict__ dst, int m0, int col0, bool skip64, float sgn,
                                        u64x4 ka, unsigned kd1, unsigned cf0, unsigned cf1) {
  const int tid = threadIdx.x, lane = tid & 63;
  STAMP_DECL
  RSTAMP(14);
  STAMP(0);
  float* stat = (float*)smem;
  const int wr = wid >> 1, wc = wid & 1;
  const int r16 = lane & 15, g = lane >> 4;
  int aoff[2][2], woff[2][4];
#pragma unroll
  for (int s = 0; s < 2; ++s)
#pragma unroll
    for (int e = 0; e < 2; ++e)
      aoff[s][e] = (64 * wr + 16 * (e ^ wc) + r16) * 128 + (((4 * s + g) ^ (r16 >> 1)) * 16);
#pragma unroll
  for (int j = 0; j < 4; ++j) {
    if constexpr (NC) {
      const int q = r16 >> 2, p = lane & 3;
      woff[0][j] = K1_W + (8 * g + q) * 384 + (((6 * wc + (j ^ wr)) ^ (q & 2) ^ (g & 1)) * 32) + p * 8;
    } else {
      woff[0][j] = K1_W + (96 * wc + 16 * ((j & 1) ^ wr) + r16) * 128 + (((4 * (j >> 1) + g) ^ (r16 >> 1)) * 16);
    }
    woff[1][j] = woff[0][j] + 2 * K1_WS;
    asm volatile("" : "+v"(woff[1][j]));
  }
  const f32x4 z4 = {0.f, 0.f, 0.f, 0.f};
  f32x4 acc[4][6], s1[2] = {z4, z4}, s2[2] = {z4, z4}, cs[3] = {z4, z4, z4};
#pragma unroll
  for (int v = 0; v < 6; ++v)
#pragma unroll
    for (int u = 0; u < 4; ++u) acc[u][v] = z4;
  bf16x8 ones;
#pragma unroll
  for (int e = 0; e < 8; ++e) ones[e] = (short)0x3F80;
  bf16x8 xa[2][4];
  s16x4 wf[2][6][2];
  auto rdn = [&](int set, int b, int n) {
    constexpr int TX[16] = {-1, -1, 0, 1, -1, -1, 2, -1, -1, 3, -1, -1, -1, -1, -1, -1};
    constexpr int TW[16] = {0, 1, -1, -1, 2, 3, -1, 4, 5, -1, 6, 7, 8, 9, 10, 11};
    if (TX[n] >= 0) {
      const int u = TX[n];
      xa[set][u] = *(const bf16x8*)(smem + aoff[set][u & 1] + (u >> 1) * 4096 + b * 16384);
    } else if constexpr (NC) {
      const int v = TW[n] >> 1, hi = TW[n] & 1;
      wf[set][v][hi] = __builtin_amdgcn_ds_read_tr16_b64_v4i16(
          (LDS_AS s16x4*)(smem + woff[b >> 1][v & 3] + (b & 1) * K1_WS + set * 12288 + hi * 1536 + (v >> 2) * 128));
    } else if (!(TW[n] & 1)) {
      const int v = TW[n] >> 1;
      const bf16x8 t = *(const bf16x8*)(smem + woff[b >> 1][2 * set + (v & 1)] + (b & 1) * K1_WS + (v >> 1) * 4096);
      wf[set][v][0] = __builtin_shufflevector(t, t, 0, 1, 2, 3);
      wf[set][v][1] = __builtin_shufflevector(t, t, 4, 5, 6, 7);
    }
  };
#define WF(set, v) __builtin_shufflevector(wf[set][v][0], wf[set][v][1], 0, 1, 2, 3, 4, 5, 6, 7)
  auto half = [&](int set, int b) {
    constexpr int OV[24] = {0, 0, 1, 1, 0, 1, 2, 2, 2, 0, 1, 2, 3, 3, 3, 3, 4, 4, 4, 4, 5, 5, 5, 5};
    constexpr int OU[24] = {0, 1, 0, 1, 2, 2, 0, 1, 2, 3, 3, 3, 0, 1, 2, 3, 0, 1, 2, 3, 0, 1, 2, 3};
    WAIT_L(NC ? 6 : 3);
#pragma unroll
    for (int n = 0; n < 24; ++n) {
      acc[OU[n]][OV[n]] = MFMA16(WF(set, OV[n]), xa[set][OU[n]], acc[OU[n]][OV[n]]);
      SCHED();
      if (n < 16) rdn(set ^ 1, b, n);
      if (n == 2 || n == 8) s1[n / 6] = MFMA16(ones, xa[set][2 * (n / 6)], s1[n / 6]);
      if (n == 5 || n == 11) s2[n / 6] = MFMA16(xa[set][2 * (n / 6)], xa[set][2 * (n / 6)], s2[n / 6]);
      if (n == 14 || n == 17 || n == 20) cs[(n - 14) / 3] = MFMA16(ones, WF(set, 2 * ((n - 14) / 3)), cs[(n - 14) / 3]);
      SCHED();
    }
  };
#pragma unroll
  for (int v = 0; v < 6; ++v) asm volatile("" : "+v"(acc[0][v]), "+v"(acc[1][v]), "+v"(acc[2][v]), "+v"(acc[3][v]));
  asm volatile("" : "+v"(s1[0]), "+v"(s1[1]), "+v"(s2[0]), "+v"(s2[1]), "+v"(cs[0]), "+v"(cs[1]), "+v"(cs[2]), "+v"(ones));
  asm volatile("s_waitcnt lgkmcnt(0)" ::"s"(ka), "s"(kd1));
  BAR();
  STAMP(2);
#pragma unroll
  for (int n = 0; n < 16; ++n) rdn(0, 0, n);
#pragma unroll 1
  for (int it = 0; it < 2; ++it) {
#pragma unroll
    for (int t = 0; t < 4; ++t) {
      SCHED();
      half(0, t);
      BAR();
      SCHED();
      half(1, (t + 1) & 3);
    }
  }
  STAMP(3);
  asm volatile("s_waitcnt vmcnt(0)" ::"v"(cf0), "v"(cf1));
  if (g == 0) {
#pragma unroll
    for (int w = 0; w < 3; ++w) stat[256 + 96 * wc + 16 * (2 * w + wr) + r16] = cs[w][0];
  }
  if (g == (r16 >> 2)) {
#pragma unroll
    for (int rg = 0; rg < 2; ++rg) {
      const float d = (lane & 2) ? ((lane & 1) ? s2[rg][3] : s2[rg][2]) : ((lane & 1) ? s2[rg][1] : s2[rg][0]);
      const float mean = s1[rg][0] * (1.f / 512.f);
      const float var = fmaxf(d * (1.f / 512.f) - mean * mean, 0.f);
      const float den = bf16_round(__builtin_amdgcn_sqrtf(bf16_round(bf16_round(var) + 1e-5f)));
      *(f32x2*)(stat + 2 * (64 * wr + 16 * (2 * rg + wc) + r16)) = f32x2{-mean, sgn * __builtin_amdgcn_rcpf(den)};
    }
  }
  WAIT_L0();
  BAR();
  STAMP(4);
  f32x2 st[4];
  float co[3];
#pragma unroll
  for (int u = 0; u < 4; ++u) st[u] = *(const f32x2*)(stat + 2 * (64 * wr + 16 * (u ^ wc) + r16));
#pragma unroll
  for (int w = 0; w < 3; ++w) co[w] = stat[256 + 96 * wc + 16 * (2 * w + (wr ^ 1)) + r16];
  bf16x8 xm[4];
#pragma unroll
  for (int u = 0; u < 4; ++u) {
    const unsigned nm = pack_bf16(st[u][0], st[u][0]);
    xm[u] = __builtin_bit_cast(bf16x8, u32x4{nm, nm & 0xffffu, 0u, 0u});
  }
  constexpr int NCH = NC ? DFF / 8 : D_ / 8;
  const __amdgpu_buffer_rsrc_t ro = mkrsrc((const char*)dst + ((size_t)(m0 >> 3) * NCH + col0 / 8) * 128);
  unsigned vo[4];
#pragma unroll
  for (int u = 0; u < 4; ++u)
    vo[u] = ((8 * wr + 2 * (u ^ wc) + (r16 >> 3)) * NCH + 12 * wc + 2 * ((g & 1) ^ wr) + (g >> 1)) * 128 + (r16 & 7) * 16;
#pragma unroll
  for (int w = 0; w < 3; ++w) {
    if (skip64 && wc == 0 && w < 2) continue;
#pragma unroll
    for (int s = 0; s < 2; ++s) {
      const bf16x8 wm = split3(s ? co[w] : cs[w][0], g != 0);
#pragma unroll
      for (int u = 0; u < 4; ++u) acc[u][2 * w + s] = MFMA16(wm, xm[u], acc[u][2 * w + s]);
    }
#pragma unroll
    for (int u = 0; u < 4; ++u) {
      unsigned pk[2][2];
#pragma unroll
      for (int s = 0; s < 2; ++s)
#pragma unroll
        for (int e = 0; e < 2; ++e) {
          unsigned t = pack_bf16(acc[u][2 * w + s][2 * e] * st[u][1], acc[u][2 * w + s][2 * e + 1] * st[u][1]);
          if constexpr (NC) t = __builtin_bit_cast(unsigned, __builtin_elementwise_max(__builtin_bit_cast(s16x2, t), s16x2{0, 0}));
          pk[s][e] = t;
        }
      const auto r0 = __builtin_amdgcn_permlane16_swap(pk[0][0], pk[1][0], false, false);
      const auto r1 = __builtin_amdgcn_permlane16_swap(pk[0][1], pk[1][1], false, false);
      __builtin_amdgcn_raw_buffer_store_b128(u32x4{r0[0], r1[0], r0[1], r1[1]}, ro, vo[u] + w * 512, 0, 16);
    }
  }
  STAMP(6);
  STAMP(7);
  STAMP_FLUSH(0);
}

__global__ __launch_bounds__(512, 2) void k1_ln_gemm1(const u16* __restrict__ x, const u16* __restrict__ wdre,
                                                      const u16* __restrict__ wdim, const u16* __restrict__ w1,
                                                      u16* __restrict__ ab, u16* __restrict__ h,
                                                      unsigned* __restrict__ cnt, const u16* __restrict__ wsre,
                                                      const u16* __restrict__ wsim, const u16* __restrict__ w2,
                                                      const u16* outp) {
  __shared__ __attribute__((aligned(1024))) char smem[K1_W + 4 * K1_WS];
  const char* code1;
  int cdist;
  u64x4 ka;
  unsigned kd1, cf0, cf1;
  asm volatile("s_getpc_b64 %0\nk1_code_mark:\n\ts_load_dwordx8 %2, %4, 0x0\n\ts_load_dword %3, %4, 0x40\n\ts_mov_b32 %1, k2_code_mark-k1_code_mark"
               : "=&s"(code1), "=&s"(cdist), "=&s"(ka), "=&s"(kd1) : "s"(__builtin_amdgcn_kernarg_segment_ptr()));
  asm volatile("v_and_b32 %0, 15, %2\n\tv_lshlrev_b32 %0, 7, %0\n\tglobal_load_dword %0, %0, %3\n\t"
               "v_and_b32 %1, 7, %2\n\tv_add_lshl_u32 %1, %1, %4, 7\n\tglobal_load_dword %1, %1, %3 offset:2048"
               : "=&v"(cf0), "=&v"(cf1) : "v"(threadIdx.x), "s"(code1), "s"(blockIdx.x & 0x78) : "memory");
  SCHED();
  const int wid = __builtin_amdgcn_readfirstlane(threadIdx.x >> 6);
  const int m0 = (8 * (blockIdx.x & 1) + (blockIdx.x >> 5)) * 128;
  if (wid < 4) {
    const Tile tl = tile_of(blockIdx.x);
    if (threadIdx.x == 0) cnt[blockIdx.x * 16] = 0;
    {
      const int xcd = blockIdx.x & 7, idx = blockIdx.x >> 3, j = threadIdx.x & 3;
      const char* p = j < 2 ? (const char*)(j ? wsim : wsre) + (1024 * (xcd & 1) + 32 * idx) * 4096 + 1024 * (xcd >> 1)
                            : (const char*)w2 + (j == 2 ? 64 * NA * (xcd >> 1) + 2 * NA * idx : KB0 + 16 * idx) * 1024;
      if (wid == 1) TOUCH64(cf0, p);
    }
    if (!tl.nc) k1_body<false>(smem, wid, ab + (size_t)tl.im * 2048 * D_, m0, tl.col0, tl.skip, tl.im ? -1.f : 1.f, ka, kd1, cf0, cf1);
    else k1_body<true>(smem, wid, h, m0, tl.col0, tl.skip, 1.f, ka, kd1, cf0, cf1);
    return;
  }
  k1_loader(smem, wid - 4, ka, m0, wsre, wsim, w2, code1 + cdist, (const char*)cnt, (const char*)outp, kd1, cf0, cf1);
}

constexpr int K2_NA = 16 + NA, K2_NT = K2_NA + NB, K2_B = 65536;

__global__ __launch_bounds__(512, 2) void k2_gemm2(const u16* __restrict__ wsre_, const u16* __restrict__ wsim_,
                                                   const u16* __restrict__ h_, const u16* __restrict__ ab_,
                                                   const u16* __restrict__ w2_, const u16* __restrict__ x,
                                                   u16* __restrict__ out, char* __restrict__ part,
                                                   unsigned* __restrict__ cnt) {
  __shared__ __attribute__((aligned(1024))) char smem[2 * K2_B];
  u64x4 ka;
  unsigned long long kw;
  unsigned kd1;
  asm volatile("k2_code_mark:\n\ts_load_dwordx8 %0, %3, 0x0\n\ts_load_dwordx2 %1, %3, 0x20\n\ts_load_dword %2, %3, 0x40"
               : "=&s"(ka), "=&s"(kw), "=&s"(kd1) : "s"(__builtin_amdgcn_kernarg_segment_ptr()));
  SCHED();
  const int tid = threadIdx.x, lane = tid & 63;
  const int wid = __builtin_amdgcn_readfirstlane(tid >> 6);
  const int bid = blockIdx.x, xcd = bid & 7, idx = bid >> 3;
  const int ksl = xcd >> 1, mt = 8 * (xcd & 1) + (idx >> 2), nt = idx & 3;
  const int ki = ksl >> 1, kj = ksl & 1;
  const int m0 = mt * 128, n0 = nt * 128;
  const int r16 = lane & 15, g = lane >> 4;
  STAMP_DECL
  RSTAMP(14);
  STAMP(0);
  if (wid < 4) {
  const int wr = wid >> 1, wc = wid & 1;
  const int q4 = r16 >> 2, p4 = lane & 3;
  int aoff[2][2], boff[4][2];
#pragma unroll
  for (int s = 0; s < 2; ++s)
#pragma unroll
    for (int i = 0; i < 2; ++i) aoff[s][i] = (64 * (i ^ ki) + 32 * wr + r16) * 128 + (((4 * s + g) ^ (r16 >> 1)) * 16);
#pragma unroll
  for (int f = 0; f < 4; ++f) {
    boff[f][0] = K2_B + g * 2048 + (8 * ((f >> 1) ^ kj) + 4 * wc + 2 * (f & 1) + (p4 >> 1)) * 128 + (q4 + 4 * (g & 1)) * 16 + 8 * (p4 & 1);
    boff[f][1] = boff[f][0] ^ 64;
  }
  const f32x4 z4 = {0.f, 0.f, 0.f, 0.f};
  f32x4 acc[4][4];
#pragma unroll
  for (int a = 0; a < 4; ++a)
#pragma unroll
    for (int b = 0; b < 4; ++b) acc[a][b] = z4;
  bf16x8 am[2][4];
  s16x4 bm[2][4][2];
  auto rdn = [&](int set, int b, int n) {
    constexpr int TA[12] = {-1, -1, 0, 1, -1, -1, 2, -1, -1, 3, -1, -1};
    constexpr int TB[12] = {0, 1, -1, -1, 2, 3, -1, 4, 5, -1, 6, 7};
    if (TA[n] >= 0) am[set][TA[n]] = *(const bf16x8*)(smem + aoff[set][TA[n] >> 1] + (TA[n] & 1) * 2048 + b * 16384);
    else
      bm[set][TB[n] >> 1][TB[n] & 1] = __builtin_amdgcn_ds_read_tr16_b64_v4i16(
          (LDS_AS s16x4*)(smem + boff[TB[n] >> 1][TB[n] & 1] + set * 8192 + b * 16384));
  };
#define BM(set, f) __builtin_shufflevector(bm[set][f][0], bm[set][f][1], 0, 1, 2, 3, 4, 5, 6, 7)
  auto half = [&](int set, int b) {
    constexpr int OB[16] = {0, 0, 1, 1, 0, 1, 2, 2, 2, 0, 1, 2, 3, 3, 3, 3};
    constexpr int OA[16] = {0, 1, 0, 1, 2, 2, 0, 1, 2, 3, 3, 3, 0, 1, 2, 3};
    WAIT_L(3);
#pragma unroll
    for (int n = 0; n < 16; ++n) {
      acc[OA[n]][OB[n]] = MFMA16(BM(set, OB[n]), am[set][OA[n]], acc[OA[n]][OB[n]]);
      SCHED();
      if (n < 12) rdn(set ^ 1, b, n);
      SCHED();
    }
  };
  auto halfb = [&](int set, int b) {
#pragma unroll
    for (int n = 0; n < 8; ++n) {
      const int i = n >> 2, bb = (n >> 1) & 1, a = n & 1;
      acc[a][bb] = MFMA16(BM(set, 2 * i + bb), am[set][2 * i + a], acc[a][bb]);
      SCHED();
      if (n < 6) { rdn(set ^ 1, b, 2 * n); rdn(set ^ 1, b, 2 * n + 1); }
      SCHED();
    }
  };
#pragma unroll
  for (int a = 0; a < 4; ++a) asm volatile("" : "+v"(acc[a][0]), "+v"(acc[a][1]), "+v"(acc[a][2]), "+v"(acc[a][3]));
  asm volatile("s_waitcnt lgkmcnt(0)" ::"s"(ka), "s"(kw), "s"(kd1));
  BAR();
  STAMP(2);
#pragma unroll
  for (int n = 0; n < 12; ++n) rdn(0, 0, n);
#pragma unroll 1
  for (int it = 0; it < 4; ++it) {
#pragma unroll
    for (int t = 0; t < 4; ++t) {
      SCHED();
      half(0, t);
      BAR();
      if (t == 3 && it == 3) {
#pragma unroll
        for (int s = 0; s < 2; ++s)
#pragma unroll
          for (int i = 0; i < 2; ++i) aoff[s][i] = (8 * (i ^ ki) + 4 * wr + (r16 >> 3)) * 1024 + (4 * s + g) * 128 + (r16 & 7) * 16;
#pragma unroll
        for (int f = 0; f < 4; ++f) {
          boff[f][0] = K2_B + (8 * g + q4) * 256 + (((8 * ((f >> 1) ^ kj) + 4 * wc + 2 * (f & 1) + (p4 >> 1)) ^ (q4 << 2) ^ ((g & 1) << 1)) * 16) + 8 * (p4 & 1);
          boff[f][1] = boff[f][0] + 1024;
        }
      }
      SCHED();
      half(1, (t + 1) & 3);
    }
  }
#pragma unroll
  for (int t = 16; t < K2_NA; ++t) {
    SCHED();
    half(0, t & 3);
    BAR();
    SCHED();
    half(1, (t + 1) & 3);
  }
  STAMP(8);
  const int tile = mt * 4 + nt;
  const int voff = wid * 2048 + lane * 16;
#pragma unroll
  for (int s = 1; s < 4; ++s) {
    const __amdgpu_buffer_rsrc_t rp = mkrsrc(part + ((tile * 4 + (ksl ^ s)) * 3 + (s - 1)) * 8192);
#pragma unroll
    for (int a = 0; a < 2; ++a) {
      const f32x4 c0 = acc[(s & 2) + a][2 * (s & 1)], c1 = acc[(s & 2) + a][2 * (s & 1) + 1];
      const u32x4 v = {pack_bf16(c0[0], c0[1]), pack_bf16(c0[2], c0[3]), pack_bf16(c1[0], c1[1]), pack_bf16(c1[2], c1[3])};
      __builtin_amdgcn_raw_buffer_store_b128(v, rp, voff + a * 1024, 0, 16);
    }
  }
  STAMP(9);
  const __amdgpu_buffer_rsrc_t rq = mkrsrc(part + (tile * 4 + ksl) * 3 * 8192);
  const size_t obase = (size_t)(m0 + 64 * ki + 32 * wr + r16) * D_ + n0 + 64 * kj + 32 * wc + 16 * (g & 1) + 8 * (g >> 1);
  GLB_AS unsigned* mine = (GLB_AS unsigned*)(cnt + (tile * 4 + ksl) * 16);
  u32x4 xv[2], pv[3][2];
  unsigned seen = 0;
#pragma unroll
  for (int t = 0; t < NB; ++t) {
    SCHED();
    halfb(0, (K2_NA + t) & 3);
    if (t == SIG_T) { STAMP(10); WAIT_V(0); STAMP(11); }
    if (t == POLL_T && wid == 0) {
      STAMP(4);
      for (int spin = 0; spin < (1 << 22) && __builtin_amdgcn_readfirstlane(seen) < 3u; ++spin) {
        __builtin_amdgcn_s_sleep(1);
        seen = __hip_atomic_load(mine, __ATOMIC_RELAXED, __HIP_MEMORY_SCOPE_AGENT);
      }
      STAMP(5);
    }
    BAR();
    if (t == SIG_T && wid == 0 && lane >= 1 && lane < 4)
      __hip_atomic_fetch_add((GLB_AS unsigned*)(cnt + (tile * 4 + (ksl ^ lane)) * 16), 1u, __ATOMIC_RELAXED, __HIP_MEMORY_SCOPE_AGENT);
    if (t == XL_T) {
#pragma unroll
      for (int a = 0; a < 2; ++a) xv[a] = *(const u32x4*)(x + obase + (size_t)16 * D_ * a);
    }
    if (t == POLL_T - POLL_D && wid == 0) seen = __hip_atomic_load(mine, __ATOMIC_RELAXED, __HIP_MEMORY_SCOPE_AGENT);
    if (t == POLL_T) {
#pragma unroll
      for (int s = 0; s < 3; ++s)
#pragma unroll
        for (int a = 0; a < 2; ++a)
          pv[s][a] = __builtin_bit_cast(u32x4, __builtin_amdgcn_raw_buffer_load_b128(rq, voff + a * 1024 + s * 8192, 0, 16));
    }
    SCHED();
    halfb(1, (K2_NA + t + 1) & 3);
  }
  STAMP(6);
#pragma unroll
  for (int a = 0; a < 2; ++a) {
    const auto x0 = __builtin_amdgcn_permlane16_swap(xv[a][0], xv[a][2], false, false);
    const auto x1 = __builtin_amdgcn_permlane16_swap(xv[a][1], xv[a][3], false, false);
    const unsigned xx[4] = {x0[0], x1[0], x0[1], x1[1]};
    unsigned o[4];
#pragma unroll
    for (int e = 0; e < 4; ++e) {
      const unsigned p0 = pv[0][a][e], p1 = pv[1][a][e], p2 = pv[2][a][e];
      const float lo = ((acc[a][e >> 1][2 * (e & 1)] + bf_lo(p0)) + (bf_lo(p1) + bf_lo(p2))) + bf_lo(xx[e]);
      const float hi = ((acc[a][e >> 1][2 * (e & 1) + 1] + bf_hi(p0)) + (bf_hi(p1) + bf_hi(p2))) + bf_hi(xx[e]);
      o[e] = pack_bf16(lo, hi);
    }
    const auto r0 = __builtin_amdgcn_permlane16_swap(o[0], o[2], false, false);
    const auto r1 = __builtin_amdgcn_permlane16_swap(o[1], o[3], false, false);
    *(u32x4*)(out + obase + (size_t)16 * D_ * a) = u32x4{r0[0], r1[0], r0[1], r1[1]};
  }
  STAMP(7);
  STAMP(12);
  STAMP_FLUSH(1);
  return;
  }
  {
    __builtin_amdgcn_s_setprio(3);
    const int lw = wid - 4;
    const int l3 = lane >> 3, l7 = lane & 7;
    unsigned va[3][4], vb[3][4];
#pragma unroll
    for (int i = 0; i < 4; ++i) {
      va[0][i] = ((m0 + 32 * lw + 8 * i + l3) * 4096 + ((l7 ^ (g & 3)) * 16)) ^ ((i & 1) << 6);
      vb[0][i] = ((2 * lw + (i >> 1)) * 64 + n0 / 8 + 8 * (i & 1) + l3) * 128 + ((l7 ^ (4 * (i >> 1))) * 16);
    }
#pragma unroll
    for (int i = 0; i < 4; ++i) asm volatile("" : "+v"(va[0][i]), "+v"(vb[0][i]));
    const char *wsre, *wsim, *h, *ab, *w2;
    asm volatile("s_waitcnt lgkmcnt(0)\n\ts_mov_b64 %0, %5\n\ts_mov_b64 %1, %6\n\ts_mov_b64 %2, %7\n\ts_mov_b64 %3, %8\n\ts_mov_b64 %4, %9"
                 : "=&s"(wsre), "=&s"(wsim), "=&s"(h), "=&s"(ab), "=&s"(w2) : "s"(ka[0]), "s"(ka[1]), "s"(ka[2]), "s"(ka[3]), "s"(kw), "s"(kd1));
    int lbuf = 0;
    auto stage = [&](const unsigned (&oa)[4], const unsigned (&ob)[4], const char* pa, const char* pb) {
      char* lb = smem + lbuf * 16384 + lw * 4096;
      lbuf = (lbuf + 1) & 3;
      const __amdgpu_buffer_rsrc_t ra = mkrsrc(pa), rb = mkrsrc(pb);
#pragma unroll
      for (int i = 0; i < 4; ++i) blds16(rb, ob[i], lb + K2_B + i * 1024);
#pragma unroll
      for (int i = 0; i < 4; ++i) blds16(ra, oa[i], lb + i * 1024);
    };
    auto run = [&](int n, const unsigned (&oa)[4], const unsigned (&ob)[4], const char* pa, const char* pb, int sa, int sb) {
#pragma unroll 1
      for (int t = 0; t < n; ++t, pa += sa, pb += sb) {
        WAIT_V(8);
        BAR();
        stage(oa, ob, pa, pb);
      }
    };
    int ks = ROT2 == 1 ? (4 * nt + 2 * (idx >> 2)) & 15 : ROT2 == 2 ? (8 * ((idx >> 2) & 1)) : ROT2 == 3 ? (4 * ((idx >> 2) & 3)) : 0;
    auto stage0 = [&]() {
      stage(va[0], vb[0], (const char*)((ks & 8) ? wsim : wsre) + 1024 * ksl + 128 * (ks & 7),
            (const char*)ab + ksl * 524288 + (ks >> 3) * 2097152 + (ks & 7) * 65536);
      ks = (ks + 1) & 15;
    };
    stage0();
    stage0();
    STAMP(1);
    WAIT_V(8);
    BAR();
    stage0();
    SCHED();
#pragma unroll
    for (int i = 0; i < 4; ++i) {
      const int c = r16 ^ (g << 2) ^ ((i >> 1) << 1);
      va[1][i] = ((m0 / 8 + 4 * lw + i) * (DFF / 8) + l3) * 128 + l7 * 16;
      vb[1][i] = (16 * lw + 4 * i + g) * 1024 + n0 * 2 + c * 16;
      va[2][i] = ((m0 / 8 + 8 * ki + 4 * (lw & 1) + i) * (DFF / 8) + 8 * (lw >> 1) + l3) * 128 + l7 * 16;
      vb[2][i] = (64 * ((c >> 3) ^ kj ^ ki) + 16 * lw + 4 * i + g) * 1024 + (n0 + 64 * kj) * 2 + (c & 7) * 16;
    }
#pragma unroll
    for (int i = 0; i < 4; ++i) asm volatile("" : "+v"(va[1][i]), "+v"(vb[1][i]), "+v"(va[2][i]), "+v"(vb[2][i]));
#pragma unroll 1
    for (int t = 0; t < 13; ++t) {
      WAIT_V(8);
      BAR();
      stage0();
    }
    STAMP(4);
    run(NA, va[1], vb[1], (const char*)h + 1024 * NA * ksl, (const char*)w2 + (size_t)65536 * NA * ksl, 1024, 65536);
    STAMP(5);
    {
      const int kb = ROT3 ? (2 * nt + (idx >> 2)) % NB : 0;
      run(NB - kb, va[2], vb[2], (const char*)h + KB0 * 16 + kb * 2048, (const char*)w2 + (size_t)KB0 * 1024 + kb * 131072, 2048, 131072);
      run(kb, va[2], vb[2], (const char*)h + KB0 * 16, (const char*)w2 + (size_t)KB0 * 1024, 2048, 131072);
    }
    STAMP(6);
    WAIT_V(8);
    BAR();
    WAIT_V(0);
    BAR();
    BAR();
    STAMP(8);
    STAMP_FLUSH(1);
  }
}

extern "C" void kernel_launch(void* const* d_in, const int* in_sizes, int n_in, void* d_out, int out_size,
                              void* d_ws, size_t ws_size, hipStream_t stream) {
  const u16* x = (const u16*)d_in[0];
  const u16* wsre = (const u16*)d_in[1];
  const u16* wsim = (const u16*)d_in[2];
  const u16* wdre = (const u16*)d_in[3];
  const u16* wdim = (const u16*)d_in[4];
  const u16* w1 = (const u16*)d_in[6];
  const u16* w2 = (const u16*)d_in[7];
  char* ws = (char*)d_ws;
  unsigned* cnt = (unsigned*)(ws + WS_CNT);
  u16* ab = (u16*)(ws + WS_AB);
  u16* h = (u16*)(ws + WS_H);
  k1_ln_gemm1<<<256, 512, 0, stream>>>(x, wdre, wdim, w1, ab, h, cnt, wsre, wsim, w2, (const u16*)d_out);
  k2_gemm2<<<256, 512, 0, stream>>>(wsre, wsim, h, ab, w2, x, (u16*)d_out, ws + WS_P, cnt);
}
```

```cpp
#include <hip/hip_runtime.h>
#include <hip/hip_bf16.h>
#include <stdint.h>

typedef __attribute__((ext_vector_type(8))) short bf16x8;
typedef __attribute__((ext_vector_type(4))) short s16x4;
typedef __attribute__((ext_vector_type(2))) short s16x2;
typedef __attribute__((ext_vector_type(4))) float f32x4;
typedef __attribute__((ext_vector_type(2))) float f32x2;
typedef __attribute__((ext_vector_type(2))) __bf16 bf16x2_t;
typedef __attribute__((ext_vector_type(4))) unsigned u32x4;
typedef __attribute__((ext_vector_type(2))) unsigned u32x2;
typedef __attribute__((ext_vector_type(4))) unsigned long long u64x4;
typedef unsigned short u16;

#define LDS_AS __attribute__((address_space(3)))
#define GLB_AS __attribute__((address_space(1)))
#define WAIT_V(n) asm volatile("s_waitcnt vmcnt(%0)" ::"n"(n) : "memory")
#define WAIT_L0() asm volatile("s_waitcnt lgkmcnt(0)" ::: "memory")
#define WAIT_L(n) __builtin_amdgcn_s_waitcnt(0xC07F | ((n) << 8))
#define BAR() do { asm volatile("" ::: "memory"); __builtin_amdgcn_s_barrier(); asm volatile("" ::: "memory"); } while (0)
#define SCHED() __builtin_amdgcn_sched_barrier(0)

#define STAMP_DECL
#define STAMP(i)
#define RSTAMP(i)
#define STAMP_FLUSH(k)

#define BLK_LAYOUT 1
constexpr int S_ = 2048, D_ = 512, DFF = 1792;
constexpr int HLD = DFF;

constexpr size_t WS_CNT = 0;
constexpr size_t WS_AB = 65536;
constexpr size_t WS_H = WS_AB + (size_t)4096 * 512 * 2;
constexpr size_t WS_P = WS_H + (size_t)2048 * 2048 * 2;

#define TSL 7, 6
#define NA 5
#define SIG_T 1
#define POLL_T 3
#define POLL_D 1
#define XL_T (POLL_T - 2)
#define ROT2 2
#define ROT3 1
#define TPAT 2
#define ST0 (tl.skip ? 6 : 2 + (idx + (idx >> 2)) % 4)
#define ST1 7
#define PRE 1
#define ZSK 1
#define MIX 1
#define PGE 1
#define TPOL ""
#define TOUCH64(d, p) asm volatile("global_load_dword %0, %1, off" TPOL : "+v"(d) : "v"(p) : "memory")
#define TOUCH(d, off, base) asm volatile("s_nop 4\n\tglobal_load_dword %0, %1, %2" : "+v"(d) : "v"(off), "s"(base) : "memory")

__device__ __forceinline__ void wait_v(int n) {
  switch (n) {
#define WV(k) case k: WAIT_V(k); break;
    WV(0) WV(1) WV(2) WV(3) WV(4) WV(5) WV(6) WV(10) WV(11) WV(12) WV(13) WV(14) WV(15) WV(16)
#undef WV
    default: WAIT_V(0);
  }
}
__device__ __forceinline__ float bf16_round(float f) { return __bfloat162float(__float2bfloat16(f)); }
__device__ __forceinline__ unsigned pack_bf16(float lo, float hi) {
  f32x2 v = {lo, hi};
  return __builtin_bit_cast(unsigned, __builtin_convertvector(v, bf16x2_t));
}
__device__ __forceinline__ float bf_lo(unsigned u) { return __builtin_bit_cast(float, u << 16); }
__device__ __forceinline__ float bf_hi(unsigned u) { return __builtin_bit_cast(float, u & 0xffff0000u); }
__device__ __forceinline__ __amdgpu_buffer_rsrc_t mkrsrc(const void* p) {
  return __builtin_amdgcn_make_buffer_rsrc((void*)p, 0, 0x7fffffff, 0x00020000);
}
__device__ __forceinline__ void blds16(__amdgpu_buffer_rsrc_t r, unsigned voff, char* l) {
  __builtin_amdgcn_raw_ptr_buffer_load_lds(r, (LDS_AS void*)l, 16, voff, 0, 0, 0);
}
__device__ __forceinline__ bf16x8 tr_read2(const char* p0, const char* p1) {
  s16x4 t0 = __builtin_amdgcn_ds_read_tr16_b64_v4i16((LDS_AS s16x4*)(p0));
  s16x4 t1 = __builtin_amdgcn_ds_read_tr16_b64_v4i16((LDS_AS s16x4*)(p1));
  return __builtin_shufflevector(t0, t1, 0, 1, 2, 3, 4, 5, 6, 7);
}
#define MFMA16(a, b, c) __builtin_amdgcn_mfma_f32_16x16x32_bf16(a, b, c, 0, 0, 0)
__device__ __forceinline__ bf16x8 split3(float c, bool z) {
  const float hi = bf16_round(c), r1 = c - hi, mid = bf16_round(r1), lo = r1 - mid;
  u32x4 v = {pack_bf16(hi, mid), pack_bf16(lo, 0.f), 0u, 0u};
  if (z) v = u32x4{0u, 0u, 0u, 0u};
  return __builtin_bit_cast(bf16x8, v);
}

constexpr int K1_W = 65536, K1_WS = 24576;
constexpr int K2_CODE_LINES = 84;
constexpr int KB0 = 4 * NA * 64;
constexpr int NB = (DFF - KB0) / 128;

struct Tile { bool nc, im, skip; int col0; };
__device__ __forceinline__ Tile tile_of(int bid) {
  const int ntile = 4 * ((bid & 7) >> 1) + ((bid >> 3) & 3);
  const unsigned long long T0 = MIX == 1 ? 0x704e838062400300ull : MIX == 2 ? 0x70834e8062034000ull : 0x4540a58380250300ull, T1 = MIX == 1 ? 0x595653a54b484525ull : MIX == 2 ? 0x5956a5534b482545ull : 0x595653704e4b4862ull;
  const unsigned e = (unsigned)((ntile < 8 ? T0 : T1) >> (8 * (ntile & 7)));
  return Tile{(e & 64) != 0, (e & 128) != 0, (e & 32) != 0, (int)(e & 31) * 64};
}

__device__ __forceinline__ void k1_loader(char* smem, int lw, u64x4 ka, int m0,
                                          const u16* pfa, const u16* pfb, const u16* pfc, const char* code2,
                                          const char* wsb, const char* outb, unsigned kd1, unsigned pfd, unsigned cf1) {
  const int tid = threadIdx.x, lane = tid & 63;
  STAMP_DECL
  RSTAMP(14);
  STAMP(0);
  const int xcd = blockIdx.x & 7, idx = blockIdx.x >> 3;
  unsigned vx[4], vw[6];
#pragma unroll
  for (int i = 0; i < 4; ++i)
    vx[i] = ((m0 + 32 * lw + 8 * i + (lane >> 3)) * (D_ * 2) + (((lane & 7) ^ ((lane >> 4) & 3)) * 16)) ^ ((i & 1) << 6);
  const char *x, *kp1, *kp2, *kp3;
  const char* wb;
  int wstep;
  auto stage_x = [&](int t) {
    const __amdgpu_buffer_rsrc_t px = mkrsrc((const char*)x + t * 128);
#pragma unroll
    for (int i = 0; i < 4; ++i) blds16(px, vx[i], smem + (t & 3) * 16384 + (4 * lw + i) * 1024);
  };
  auto stage_w = [&](int t) {
    const __amdgpu_buffer_rsrc_t pw = mkrsrc(wb + t * wstep);
#pragma unroll
    for (int i = 0; i < 6; ++i) blds16(pw, vw[i], smem + K1_W + (t & 3) * K1_WS + (6 * lw + i) * 1024);
  };
  SCHED();
  int b2 = blockIdx.x;
  asm volatile("" : "+s"(b2));
  const Tile tl = tile_of(b2);
  const bool nc = tl.nc;
  wstep = nc ? 64 * (DFF * 2) : 128;
  __builtin_amdgcn_s_setprio(3);
  if (nc) {
#pragma unroll
    for (int i = 0; i < 3; ++i) {
      const int id = 64 * i + lane, kr = id / 24, c = id % 24;
      vw[i] = (16 * lw + kr) * (DFF * 2) + ((c ^ (((kr >> 1) & 1) << 2)) * 16);
      if (ZSK && tl.skip && c < 8) vw[i] = 0x80000000u;
      vw[i + 3] = (vw[i] ^ 32) + 8 * (DFF * 2);
    }
  } else {
#pragma unroll
    for (int i = 0; i < 6; ++i)
      vw[i] = (((48 * lw + 8 * i + (lane >> 3)) * (D_ * 2) + (((lane & 7) ^ ((lane >> 4) & 3)) * 16)) ^ ((i & 1) << 6)) |
              (ZSK && tl.skip && 48 * lw + 8 * i < 64 ? 0x80000000u : 0u);
  }
#pragma unroll
  for (int i = 0; i < 6; ++i) asm volatile("" : "+v"(vw[i]));
  int wo = nc ? tl.col0 * 2 : tl.col0 * (D_ * 2);
  asm volatile("" : "+s"(wo));
  asm volatile("" : "+v"(vx[0]), "+v"(vx[1]), "+v"(vx[2]), "+v"(vx[3]));
  asm volatile("s_waitcnt lgkmcnt(0)\n\ts_mov_b64 %0, %4\n\ts_mov_b64 %1, %5\n\ts_mov_b64 %2, %6\n\ts_mov_b64 %3, %7"
               : "=&s"(x), "=&s"(kp1), "=&s"(kp2), "=&s"(kp3) : "s"(ka[0]), "s"(ka[1]), "s"(ka[2]), "s"(ka[3]), "s"(kd1));
  stage_x(0);
  wb = (nc ? kp3 : tl.im ? kp2 : kp1) + wo;
  stage_w(0);
  stage_x(1);
  stage_w(1);
  SCHED();
  const char* ta[4];
  {
    const int ksl = xcd >> 1, t2 = tid - 256;
    constexpr int LA = 2 * NA * 8, LB = (DFF - KB0) / 32 * 4;
    const int c = (2 * (idx >> 2) + 4 * (t2 & 1) + ((t2 >> 1) & 1)) & 7;
    ta[0] = (const char*)((t2 & 4) ? pfb : pfa) + (1024 * (xcd & 1) + 32 * idx + (t2 >> 3)) * 4096 + 1024 * ksl + c * 128;
    ta[1] = (const char*)((t2 & 4) ? pfb : pfa) + (1024 * (xcd & 1) + 32 * idx + (t2 >> 3)) * 4096 + 1024 * ksl + (c ^ 2) * 128;
    ta[0] = (const char*)((t2 & 128) ? pfb : pfa) + (1024 * (xcd & 1) + 32 * idx + ((t2 >> 3) & 15)) * 4096 + 1024 * ksl + (t2 & 7) * 128;
    ta[1] = ta[0] + 16 * 4096;
    const int tb = min(max(t2 - LA, 0), LB - 1);
    ta[2] = (const char*)pfc + (t2 < LA ? (64 * NA * ksl + 2 * NA * idx + (t2 >> 3)) * 1024 + (t2 & 7) * 128
                                        : (KB0 + (LB / 4) * idx + (tb >> 2)) * 1024 + (tb & 3) * 256 + (ksl & 1) * 128);
    const int j = min(11 * lw + min(lane, 10), 43);
    ta[3] = j < 37 ? wsb + 32768 + j * 524288 : j < 41 ? outb + (j - 37) * 524288
                   : code2 + min(3 * idx + j - 41, K2_CODE_LINES - 1) * 128;
  }
  constexpr int TP[2] = {TSL};
  const int tp0 = ST0, tp1 = ST1;
  auto ntouch = [&](int sg) { return (TP[0] == sg) + (TP[1] == sg); };
  asm volatile("" : "+v"(ta[0]), "+v"(ta[1]), "+v"(ta[2]), "+v"(ta[3]));
  STAMP(1);
  WAIT_V(10);
  asm volatile("" ::"v"(cf1));
  BAR();
  STAMP(2);
  stage_x(2);
  stage_w(2);
  if (tp0 == 2) TOUCH64(pfd, ta[0]);
#pragma unroll
  for (int t = 0; t < 8; ++t) {
    if (t < 7) {
      const int n = t + 1 < 7 ? ntouch(t + 1) + 10 + ntouch(t + 2) : ntouch(7);
      const int d = (tp0 == t + 1) + (tp0 == t + 2) + (tp1 == t + 1) + (tp1 == t + 2);
      if (d == 0) wait_v(n);
      else if (d == 1) wait_v(n + 1);
      else wait_v(n + 2);
    }
    BAR();
    if (t + 3 < 8) {
      stage_x(t + 3);
      stage_w(t + 3);
      if (TP[1] == t + 3) TOUCH64(pfd, ta[3]);
      if (tp0 == t + 3) TOUCH64(pfd, ta[0]);
      if (tp1 == t + 3) TOUCH64(pfd, ta[1]);
      if (TP[0] == t + 3) TOUCH64(pfd, ta[2]);
    }
  }
  STAMP(3);
  BAR();
  WAIT_V(0);
  asm volatile("" ::"v"(pfd));
  STAMP(12);
  STAMP_FLUSH(0);
}

template <bool NC>
__device__ __forceinline__ void k1_body(char* smem, int wid, u16* __restrict__ dst, int m0, int col0, bool skip64, float sgn,
                                        u64x4 ka, unsigned kd1, unsigned cf0, unsigned cf1) {
  const int tid = threadIdx.x, lane = tid & 63;
  STAMP_DECL
  RSTAMP(14);
  STAMP(0);
  float* stat = (float*)smem;
  const int wr = wid >> 1, wc = wid & 1;
  const int r16 = lane & 15, g = lane >> 4;
  int aoff[2][2], woff[2][4];
#pragma unroll
  for (int s = 0; s < 2; ++s)
#pragma unroll
    for (int e = 0; e < 2; ++e)
      aoff[s][e] = (64 * wr + 16 * (e ^ wc) + r16) * 128 + (((4 * s + g) ^ (r16 >> 1)) * 16);
#pragma unroll
  for (int j = 0; j < 4; ++j) {
    if constexpr (NC) {
      const int q = r16 >> 2, p = lane & 3;
      woff[0][j] = K1_W + (8 * g + q) * 384 + (((6 * wc + (j ^ wr)) ^ (q & 2) ^ (g & 1)) * 32) + p * 8;
    } else {
      woff[0][j] = K1_W + (96 * wc + 16 * ((j & 1) ^ wr) + r16) * 128 + (((4 * (j >> 1) + g) ^ (r16 >> 1)) * 16);
    }
    woff[1][j] = woff[0][j] + 2 * K1_WS;
    asm volatile("" : "+v"(woff[1][j]));
  }
  const f32x4 z4 = {0.f, 0.f, 0.f, 0.f};
  f32x4 acc[4][6], s1[2] = {z4, z4}, s2[2] = {z4, z4}, cs[3] = {z4, z4, z4};
#pragma unroll
  for (int v = 0; v < 6; ++v)
#pragma unroll
    for (int u = 0; u < 4; ++u) acc[u][v] = z4;
  bf16x8 ones;
#pragma unroll
  for (int e = 0; e < 8; ++e) ones[e] = (short)0x3F80;
  bf16x8 xa[2][4];
  s16x4 wf[2][6][2];
  auto rdn = [&](int set, int b, int n) {
    constexpr int TX[16] = {-1, -1, 0, 1, -1, -1, 2, -1, -1, 3, -1, -1, -1, -1, -1, -1};
    constexpr int TW[16] = {0, 1, -1, -1, 2, 3, -1, 4, 5, -1, 6, 7, 8, 9, 10, 11};
    if (TX[n] >= 0) {
      const int u = TX[n];
      xa[set][u] = *(const bf16x8*)(smem + aoff[set][u & 1] + (u >> 1) * 4096 + b * 16384);
    } else if constexpr (NC) {
      const int v = TW[n] >> 1, hi = TW[n] & 1;
      wf[set][v][hi] = __builtin_amdgcn_ds_read_tr16_b64_v4i16(
          (LDS_AS s16x4*)(smem + woff[b >> 1][v & 3] + (b & 1) * K1_WS + set * 12288 + hi * 1536 + (v >> 2) * 128));
    } else if (!(TW[n] & 1)) {
      const int v = TW[n] >> 1;
      const bf16x8 t = *(const bf16x8*)(smem + woff[b >> 1][2 * set + (v & 1)] + (b & 1) * K1_WS + (v >> 1) * 4096);
      wf[set][v][0] = __builtin_shufflevector(t, t, 0, 1, 2, 3);
      wf[set][v][1] = __builtin_shufflevector(t, t, 4, 5, 6, 7);
    }
  };
#define WF(set, v) __builtin_shufflevector(wf[set][v][0], wf[set][v][1], 0, 1, 2, 3, 4, 5, 6, 7)
  auto half = [&](int set, int b) {
    constexpr int OV[24] = {0, 0, 1, 1, 0, 1, 2, 2, 2, 0, 1, 2, 3, 3, 3, 3, 4, 4, 4, 4, 5, 5, 5, 5};
    constexpr int OU[24] = {0, 1, 0, 1, 2, 2, 0, 1, 2, 3, 3, 3, 0, 1, 2, 3, 0, 1, 2, 3, 0, 1, 2, 3};
    WAIT_L(NC ? 6 : 3);
#pragma unroll
    for (int n = 0; n < 24; ++n) {
      acc[OU[n]][OV[n]] = MFMA16(WF(set, OV[n]), xa[set][OU[n]], acc[OU[n]][OV[n]]);
      SCHED();
      if (n < 16) rdn(set ^ 1, b, n);
      if (n == 2 || n == 8) s1[n / 6] = MFMA16(ones, xa[set][2 * (n / 6)], s1[n / 6]);
      if (n == 5 || n == 11) s2[n / 6] = MFMA16(xa[set][2 * (n / 6)], xa[set][2 * (n / 6)], s2[n / 6]);
      if (n == 14 || n == 17 || n == 20) cs[(n - 14) / 3] = MFMA16(ones, WF(set, 2 * ((n - 14) / 3)), cs[(n - 14) / 3]);
      SCHED();
    }
  };
#pragma unroll
  for (int v = 0; v < 6; ++v) asm volatile("" : "+v"(acc[0][v]), "+v"(acc[1][v]), "+v"(acc[2][v]), "+v"(acc[3][v]));
  asm volatile("" : "+v"(s1[0]), "+v"(s1[1]), "+v"(s2[0]), "+v"(s2[1]), "+v"(cs[0]), "+v"(cs[1]), "+v"(cs[2]), "+v"(ones));
  asm volatile("s_waitcnt lgkmcnt(0)" ::"s"(ka), "s"(kd1));
  BAR();
  STAMP(2);
#pragma unroll
  for (int n = 0; n < 16; ++n) rdn(0, 0, n);
#pragma unroll 1
  for (int it = 0; it < 2; ++it) {
#pragma unroll
    for (int t = 0; t < 4; ++t) {
      SCHED();
      half(0, t);
      BAR();
      SCHED();
      half(1, (t + 1) & 3);
    }
  }
  STAMP(3);
  asm volatile("s_waitcnt vmcnt(0)" ::"v"(cf0), "v"(cf1));
  if (g == 0) {
#pragma unroll
    for (int w = 0; w < 3; ++w) stat[256 + 96 * wc + 16 * (2 * w + wr) + r16] = cs[w][0];
  }
  if (g == (r16 >> 2)) {
#pragma unroll
    for (int rg = 0; rg < 2; ++rg) {
      const float d = (lane & 2) ? ((lane & 1) ? s2[rg][3] : s2[rg][2]) : ((lane & 1) ? s2[rg][1] : s2[rg][0]);
      const float mean = s1[rg][0] * (1.f / 512.f);
      const float var = fmaxf(d * (1.f / 512.f) - mean * mean, 0.f);
      const float den = bf16_round(__builtin_amdgcn_sqrtf(bf16_round(bf16_round(var) + 1e-5f)));
      *(f32x2*)(stat + 2 * (64 * wr + 16 * (2 * rg + wc) + r16)) = f32x2{-mean, sgn * __builtin_amdgcn_rcpf(den)};
    }
  }
  WAIT_L0();
  BAR();
  STAMP(4);
  f32x2 st[4];
  float co[3];
#pragma unroll
  for (int u = 0; u < 4; ++u) st[u] = *(const f32x2*)(stat + 2 * (64 * wr + 16 * (u ^ wc) + r16));
#pragma unroll
  for (int w = 0; w < 3; ++w) co[w] = stat[256 + 96 * wc + 16 * (2 * w + (wr ^ 1)) + r16];
  bf16x8 xm[4];
#pragma unroll
  for (int u = 0; u < 4; ++u) {
    const unsigned nm = pack_bf16(st[u][0], st[u][0]);
    xm[u] = __builtin_bit_cast(bf16x8, u32x4{nm, nm & 0xffffu, 0u, 0u});
  }
  constexpr int NCH = NC ? DFF / 8 : D_ / 8;
  const __amdgpu_buffer_rsrc_t ro = mkrsrc((const char*)dst + ((size_t)(m0 >> 3) * NCH + col0 / 8) * 128);
  unsigned vo[4];
#pragma unroll
  for (int u = 0; u < 4; ++u)
    vo[u] = ((8 * wr + 2 * (u ^ wc) + (r16 >> 3)) * NCH + 12 * wc + 2 * ((g & 1) ^ wr) + (g >> 1)) * 128 + (r16 & 7) * 16;
#pragma unroll
  for (int w = 0; w < 3; ++w) {
    if (skip64 && wc == 0 && w < 2) continue;
#pragma unroll
    for (int s = 0; s < 2; ++s) {
      const bf16x8 wm = split3(s ? co[w] : cs[w][0], g != 0);
#pragma unroll
      for (int u = 0; u < 4; ++u) acc[u][2 * w + s] = MFMA16(wm, xm[u], acc[u][2 * w + s]);
    }
#pragma unroll
    for (int u = 0; u < 4; ++u) {
      unsigned pk[2][2];
#pragma unroll
      for (int s = 0; s < 2; ++s)
#pragma unroll
        for (int e = 0; e < 2; ++e) {
          unsigned t = pack_bf16(acc[u][2 * w + s][2 * e] * st[u][1], acc[u][2 * w + s][2 * e + 1] * st[u][1]);
          if constexpr (NC) t = __builtin_bit_cast(unsigned, __builtin_elementwise_max(__builtin_bit_cast(s16x2, t), s16x2{0, 0}));
          pk[s][e] = t;
        }
      const auto r0 = __builtin_amdgcn_permlane16_swap(pk[0][0], pk[1][0], false, false);
      const auto r1 = __builtin_amdgcn_permlane16_swap(pk[0][1], pk[1][1], false, false);
      __builtin_amdgcn_raw_buffer_store_b128(u32x4{r0[0], r1[0], r0[1], r1[1]}, ro, vo[u] + w * 512, 0, 16);
    }
  }
  STAMP(6);
  STAMP(7);
  STAMP_FLUSH(0);
}

__global__ __launch_bounds__(512, 2) void k1_ln_gemm1(const u16* __restrict__ x, const u16* __restrict__ wdre,
                                                      const u16* __restrict__ wdim, const u16* __restrict__ w1,
                                                      u16* __restrict__ ab, u16* __restrict__ h,
                                                      unsigned* __restrict__ cnt, const u16* __restrict__ wsre,
                                                      const u16* __restrict__ wsim, const u16* __restrict__ w2,
                                                      const u16* outp) {
  __shared__ __attribute__((aligned(1024))) char smem[K1_W + 4 * K1_WS];
  const char* code1;
  int cdist;
  u64x4 ka;
  unsigned kd1, cf0, cf1;
  asm volatile("s_getpc_b64 %0\nk1_code_mark:\n\ts_load_dwordx8 %2, %4, 0x0\n\ts_load_dword %3, %4, 0x40\n\ts_mov_b32 %1, k2_code_mark-k1_code_mark"
               : "=&s"(code1), "=&s"(cdist), "=&s"(ka), "=&s"(kd1) : "s"(__builtin_amdgcn_kernarg_segment_ptr()));
  asm volatile("v_and_b32 %0, 15, %2\n\tv_lshlrev_b32 %0, 7, %0\n\tglobal_load_dword %0, %0, %3\n\t"
               "v_and_b32 %1, 7, %2\n\tv_add_lshl_u32 %1, %1, %4, 7\n\tglobal_load_dword %1, %1, %3 offset:2048"
               : "=&v"(cf0), "=&v"(cf1) : "v"(threadIdx.x), "s"(code1), "s"(blockIdx.x & 0x78) : "memory");
  SCHED();
  const int wid = __builtin_amdgcn_readfirstlane(threadIdx.x >> 6);
  const int m0 = (8 * (blockIdx.x & 1) + (blockIdx.x >> 5)) * 128;
  if (wid < 4) {
    const Tile tl = tile_of(blockIdx.x);
    if (threadIdx.x == 0) cnt[blockIdx.x * 16] = 0;
    {
      const int xcd = blockIdx.x & 7, idx = blockIdx.x >> 3, j = threadIdx.x & 3;
      const char* p = j < 2 ? (const char*)(j ? wsim : wsre) + (1024 * (xcd & 1) + 32 * idx) * 4096 + 1024 * (xcd >> 1)
                            : (const char*)w2 + (j == 2 ? 64 * NA * (xcd >> 1) + 2 * NA * idx : KB0 + 16 * idx) * 1024;
      if (wid == 1) TOUCH64(cf0, p);
    }
    if (!tl.nc) k1_body<false>(smem, wid, ab + (size_t)tl.im * 2048 * D_, m0, tl.col0, tl.skip, tl.im ? -1.f : 1.f, ka, kd1, cf0, cf1);
    else k1_body<true>(smem, wid, h, m0, tl.col0, tl.skip, 1.f, ka, kd1, cf0, cf1);
    return;
  }
  k1_loader(smem, wid - 4, ka, m0, wsre, wsim, w2, code1 + cdist, (const char*)cnt, (const char*)outp, kd1, cf0, cf1);
}

constexpr int K2_NA = 16 + NA, K2_NT = K2_NA + NB, K2_B = 65536;

__global__ __launch_bounds__(512, 2) void k2_gemm2(const u16* __restrict__ wsre_, const u16* __restrict__ wsim_,
                                                   const u16* __restrict__ h_, const u16* __restrict__ ab_,
                                                   const u16* __restrict__ w2_, const u16* __restrict__ x,
                                                   u16* __restrict__ out, char* __restrict__ part,
                                                   unsigned* __restrict__ cnt) {
  __shared__ __attribute__((aligned(1024))) char smem[2 * K2_B];
  u64x4 ka;
  unsigned long long kw;
  unsigned kd1;
  asm volatile("k2_code_mark:\n\ts_load_dwordx8 %0, %3, 0x0\n\ts_load_dwordx2 %1, %3, 0x20\n\ts_load_dword %2, %3, 0x40"
               : "=&s"(ka), "=&s"(kw), "=&s"(kd1) : "s"(__builtin_amdgcn_kernarg_segment_ptr()));
  SCHED();
  const int tid = threadIdx.x, lane = tid & 63;
  const int wid = __builtin_amdgcn_readfirstlane(tid >> 6);
  const int bid = blockIdx.x, xcd = bid & 7, idx = bid >> 3;
  const int ksl = xcd >> 1, mt = 8 * (xcd & 1) + (idx >> 2), nt = idx & 3;
  const int ki = ksl >> 1, kj = ksl & 1;
  const int m0 = mt * 128, n0 = nt * 128;
  const int r16 = lane & 15, g = lane >> 4;
  STAMP_DECL
  RSTAMP(14);
  STAMP(0);
  if (wid < 4) {
  const int wr = wid >> 1, wc = wid & 1;
  const int q4 = r16 >> 2, p4 = lane & 3;
  int aoff[2][2], boff[4][2];
#pragma unroll
  for (int s = 0; s < 2; ++s)
#pragma unroll
    for (int i = 0; i < 2; ++i) aoff[s][i] = (64 * (i ^ ki) + 32 * wr + r16) * 128 + (((4 * s + g) ^ (r16 >> 1)) * 16);
#pragma unroll
  for (int f = 0; f < 4; ++f) {
    boff[f][0] = K2_B + g * 2048 + (8 * ((f >> 1) ^ kj) + 4 * wc + 2 * (f & 1) + (p4 >> 1)) * 128 + (q4 + 4 * (g & 1)) * 16 + 8 * (p4 & 1);
    boff[f][1] = boff[f][0] ^ 64;
  }
  const f32x4 z4 = {0.f, 0.f, 0.f, 0.f};
  f32x4 acc[4][4];
#pragma unroll
  for (int a = 0; a < 4; ++a)
#pragma unroll
    for (int b = 0; b < 4; ++b) acc[a][b] = z4;
  bf16x8 am[2][4];
  s16x4 bm[2][4][2];
  auto rdn = [&](int set, int b, int n) {
    constexpr int TA[12] = {-1, -1, 0, 1, -1, -1, 2, -1, -1, 3, -1, -1};
    constexpr int TB[12] = {0, 1, -1, -1, 2, 3, -1, 4, 5, -1, 6, 7};
    if (TA[n] >= 0) am[set][TA[n]] = *(const bf16x8*)(smem + aoff[set][TA[n] >> 1] + (TA[n] & 1) * 2048 + b * 16384);
    else
      bm[set][TB[n] >> 1][TB[n] & 1] = __builtin_amdgcn_ds_read_tr16_b64_v4i16(
          (LDS_AS s16x4*)(smem + boff[TB[n] >> 1][TB[n] & 1] + set * 8192 + b * 16384));
  };
#define BM(set, f) __builtin_shufflevector(bm[set][f][0], bm[set][f][1], 0, 1, 2, 3, 4, 5, 6, 7)
  auto half = [&](int set, int b) {
    constexpr int OB[16] = {0, 0, 1, 1, 0, 1, 2, 2, 2, 0, 1, 2, 3, 3, 3, 3};
    constexpr int OA[16] = {0, 1, 0, 1, 2, 2, 0, 1, 2, 3, 3, 3, 0, 1, 2, 3};
    WAIT_L(3);
#pragma unroll
    for (int n = 0; n < 16; ++n) {
      acc[OA[n]][OB[n]] = MFMA16(BM(set, OB[n]), am[set][OA[n]], acc[OA[n]][OB[n]]);
      SCHED();
      if (n < 12) rdn(set ^ 1, b, n);
      SCHED();
    }
  };
  auto halfb = [&](int set, int b) {
#pragma unroll
    for (int n = 0; n < 8; ++n) {
      const int i = n >> 2, bb = (n >> 1) & 1, a = n & 1;
      acc[a][bb] = MFMA16(BM(set, 2 * i + bb), am[set][2 * i + a], acc[a][bb]);
      SCHED();
      if (n < 6) { rdn(set ^ 1, b, 2 * n); rdn(set ^ 1, b, 2 * n + 1); }
      SCHED();
    }
  };
#pragma unroll
  for (int a = 0; a < 4; ++a) asm volatile("" : "+v"(acc[a][0]), "+v"(acc[a][1]), "+v"(acc[a][2]), "+v"(acc[a][3]));
  asm volatile("s_waitcnt lgkmcnt(0)" ::"s"(ka), "s"(kw), "s"(kd1));
  BAR();
  STAMP(2);
#pragma unroll
  for (int n = 0; n < 12; ++n) rdn(0, 0, n);
#pragma unroll 1
  for (int it = 0; it < 4; ++it) {
#pragma unroll
    for (int t = 0; t < 4; ++t) {
      SCHED();
      half(0, t);
      BAR();
      if (t == 3 && it == 3) {
#pragma unroll
        for (int s = 0; s < 2; ++s)
#pragma unroll
          for (int i = 0; i < 2; ++i) aoff[s][i] = (8 * (i ^ ki) + 4 * wr + (r16 >> 3)) * 1024 + (4 * s + g) * 128 + (r16 & 7) * 16;
#pragma unroll
        for (int f = 0; f < 4; ++f) {
          boff[f][0] = K2_B + (8 * g + q4) * 256 + (((8 * ((f >> 1) ^ kj) + 4 * wc + 2 * (f & 1) + (p4 >> 1)) ^ (q4 << 2) ^ ((g & 1) << 1)) * 16) + 8 * (p4 & 1);
          boff[f][1] = boff[f][0] + 1024;
        }
      }
      SCHED();
      half(1, (t + 1) & 3);
    }
  }
#pragma unroll
  for (int t = 16; t < K2_NA; ++t) {
    SCHED();
    half(0, t & 3);
    BAR();
    SCHED();
    half(1, (t + 1) & 3);
  }
  STAMP(8);
  const int tile = mt * 4 + nt;
  const int voff = wid * 2048 + lane * 16;
#pragma unroll
  for (int s = 1; s < 4; ++s) {
    const __amdgpu_buffer_rsrc_t rp = mkrsrc(part + ((tile * 4 + (ksl ^ s)) * 3 + (s - 1)) * 8192);
#pragma unroll
    for (int a = 0; a < 2; ++a) {
      const f32x4 c0 = acc[(s & 2) + a][2 * (s & 1)], c1 = acc[(s & 2) + a][2 * (s & 1) + 1];
      const u32x4 v = {pack_bf16(c0[0], c0[1]), pack_bf16(c0[2], c0[3]), pack_bf16(c1[0], c1[1]), pack_bf16(c1[2], c1[3])};
      __builtin_amdgcn_raw_buffer_store_b128(v, rp, voff + a * 1024, 0, 16);
    }
  }
  STAMP(9);
  const __amdgpu_buffer_rsrc_t rq = mkrsrc(part + (tile * 4 + ksl) * 3 * 8192);
  const size_t obase = (size_t)(m0 + 64 * ki + 32 * wr + r16) * D_ + n0 + 64 * kj + 32 * wc + 16 * (g & 1) + 8 * (g >> 1);
  GLB_AS unsigned* mine = (GLB_AS unsigned*)(cnt + (tile * 4 + ksl) * 16);
  u32x4 xv[2], pv[3][2];
  unsigned seen = 0;
#pragma unroll
  for (int t = 0; t < NB; ++t) {
    SCHED();
    halfb(0, (K2_NA + t) & 3);
    if (t == SIG_T) { STAMP(10); WAIT_V(0); STAMP(11); }
    if (t == POLL_T && wid == 0) {
      STAMP(4);
      for (int spin = 0; spin < (1 << 22) && __builtin_amdgcn_readfirstlane(seen) < 3u; ++spin) {
        __builtin_amdgcn_s_sleep(1);
        seen = __hip_atomic_load(mine, __ATOMIC_RELAXED, __HIP_MEMORY_SCOPE_AGENT);
      }
      STAMP(5);
    }
    BAR();
    if (t == SIG_T && wid == 0 && lane >= 1 && lane < 4)
      __hip_atomic_fetch_add((GLB_AS unsigned*)(cnt + (tile * 4 + (ksl ^ lane)) * 16), 1u, __ATOMIC_RELAXED, __HIP_MEMORY_SCOPE_AGENT);
    if (t == XL_T) {
#pragma unroll
      for (int a = 0; a < 2; ++a) xv[a] = *(const u32x4*)(x + obase + (size_t)16 * D_ * a);
    }
    if (t == POLL_T - POLL_D && wid == 0) seen = __hip_atomic_load(mine, __ATOMIC_RELAXED, __HIP_MEMORY_SCOPE_AGENT);
    if (t == POLL_T) {
#pragma unroll
      for (int s = 0; s < 3; ++s)
#pragma unroll
        for (int a = 0; a < 2; ++a)
          pv[s][a] = __builtin_bit_cast(u32x4, __builtin_amdgcn_raw_buffer_load_b128(rq, voff + a * 1024 + s * 8192, 0, 16));
    }
    SCHED();
    halfb(1, (K2_NA + t + 1) & 3);
  }
  STAMP(6);
#pragma unroll
  for (int a = 0; a < 2; ++a) {
    const auto x0 = __builtin_amdgcn_permlane16_swap(xv[a][0], xv[a][2], false, false);
    const auto x1 = __builtin_amdgcn_permlane16_swap(xv[a][1], xv[a][3], false, false);
    const unsigned xx[4] = {x0[0], x1[0], x0[1], x1[1]};
    unsigned o[4];
#pragma unroll
    for (int e = 0; e < 4; ++e) {
      const unsigned p0 = pv[0][a][e], p1 = pv[1][a][e], p2 = pv[2][a][e];
      const float lo = ((acc[a][e >> 1][2 * (e & 1)] + bf_lo(p0)) + (bf_lo(p1) + bf_lo(p2))) + bf_lo(xx[e]);
      const float hi = ((acc[a][e >> 1][2 * (e & 1) + 1] + bf_hi(p0)) + (bf_hi(p1) + bf_hi(p2))) + bf_hi(xx[e]);
      o[e] = pack_bf16(lo, hi);
    }
    const auto r0 = __builtin_amdgcn_permlane16_swap(o[0], o[2], false, false);
    const auto r1 = __builtin_amdgcn_permlane16_swap(o[1], o[3], false, false);
    *(u32x4*)(out + obase + (size_t)16 * D_ * a) = u32x4{r0[0], r1[0], r0[1], r1[1]};
  }
  STAMP(7);
  STAMP(12);
  STAMP_FLUSH(1);
  return;
  }
  {
    __builtin_amdgcn_s_setprio(3);
    const int lw = wid - 4;
    const int l3 = lane >> 3, l7 = lane & 7;
    unsigned va[3][4], vb[3][4];
#pragma unroll
    for (int i = 0; i < 4; ++i) {
      va[0][i] = ((m0 + 32 * lw + 8 * i + l3) * 4096 + ((l7 ^ (g & 3)) * 16)) ^ ((i & 1) << 6);
      vb[0][i] = ((2 * lw + (i >> 1)) * 64 + n0 / 8 + 8 * (i & 1) + l3) * 128 + ((l7 ^ (4 * (i >> 1))) * 16);
    }
#pragma unroll
    for (int i = 0; i < 4; ++i) asm volatile("" : "+v"(va[0][i]), "+v"(vb[0][i]));
    const char *wsre, *wsim, *h, *ab, *w2;
    asm volatile("s_waitcnt lgkmcnt(0)\n\ts_mov_b64 %0, %5\n\ts_mov_b64 %1, %6\n\ts_mov_b64 %2, %7\n\ts_mov_b64 %3, %8\n\ts_mov_b64 %4, %9"
                 : "=&s"(wsre), "=&s"(wsim), "=&s"(h), "=&s"(ab), "=&s"(w2) : "s"(ka[0]), "s"(ka[1]), "s"(ka[2]), "s"(ka[3]), "s"(kw), "s"(kd1));
    int lbuf = 0;
    auto stage = [&](const unsigned (&oa)[4], const unsigned (&ob)[4], const char* pa, const char* pb) {
      char* lb = smem + lbuf * 16384 + lw * 4096;
      lbuf = (lbuf + 1) & 3;
      const __amdgpu_buffer_rsrc_t ra = mkrsrc(pa), rb = mkrsrc(pb);
#pragma unroll
      for (int i = 0; i < 4; ++i) blds16(rb, ob[i], lb + K2_B + i * 1024);
#pragma unroll
      for (int i = 0; i < 4; ++i) blds16(ra, oa[i], lb + i * 1024);
    };
    auto run = [&](int n, const unsigned (&oa)[4], const unsigned (&ob)[4], const char* pa, const char* pb, int sa, int sb) {
#pragma unroll 1
      for (int t = 0; t < n; ++t, pa += sa, pb += sb) {
        WAIT_V(8);
        BAR();
        stage(oa, ob, pa, pb);
      }
    };
    int ks = ROT2 == 1 ? (4 * nt + 2 * (idx >> 2)) & 15 : ROT2 == 2 ? (8 * ((idx >> 2) & 1)) : ROT2 == 3 ? (4 * ((idx >> 2) & 3)) : 0;
    auto stage0 = [&]() {
      stage(va[0], vb[0], (const char*)((ks & 8) ? wsim : wsre) + 1024 * ksl + 128 * (ks & 7),
            (const char*)ab + ksl * 524288 + (ks >> 3) * 2097152 + (ks & 7) * 65536);
      ks = (ks + 1) & 15;
    };
    stage0();
    stage0();
    STAMP(1);
    WAIT_V(8);
    BAR();
    stage0();
    SCHED();
#pragma unroll
    for (int i = 0; i < 4; ++i) {
      const int c = r16 ^ (g << 2) ^ ((i >> 1) << 1);
      va[1][i] = ((m0 / 8 + 4 * lw + i) * (DFF / 8) + l3) * 128 + l7 * 16;
      vb[1][i] = (16 * lw + 4 * i + g) * 1024 + n0 * 2 + c * 16;
      va[2][i] = ((m0 / 8 + 8 * ki + 4 * (lw & 1) + i) * (DFF / 8) + 8 * (lw >> 1) + l3) * 128 + l7 * 16;
      vb[2][i] = (64 * ((c >> 3) ^ kj ^ ki) + 16 * lw + 4 * i + g) * 1024 + (n0 + 64 * kj) * 2 + (c & 7) * 16;
    }
#pragma unroll
    for (int i = 0; i < 4; ++i) asm volatile("" : "+v"(va[1][i]), "+v"(vb[1][i]), "+v"(va[2][i]), "+v"(vb[2][i]));
#pragma unroll 1
    for (int t = 0; t < 13; ++t) {
      WAIT_V(8);
      BAR();
      stage0();
    }
    STAMP(4);
    run(NA, va[1], vb[1], (const char*)h + 1024 * NA * ksl, (const char*)w2 + (size_t)65536 * NA * ksl, 1024, 65536);
    STAMP(5);
    {
      const int kb = ROT3 ? (2 * nt + (idx >> 2)) % NB : 0;
      run(NB - kb, va[2], vb[2], (const char*)h + KB0 * 16 + kb * 2048, (const char*)w2 + (size_t)KB0 * 1024 + kb * 131072, 2048, 131072);
      run(kb, va[2], vb[2], (const char*)h + KB0 * 16, (const char*)w2 + (size_t)KB0 * 1024, 2048, 131072);
    }
    STAMP(6);
    WAIT_V(8);
    BAR();
    WAIT_V(0);
    BAR();
    BAR();
    STAMP(8);
    STAMP_FLUSH(1);
  }
}

extern "C" void kernel_launch(void* const* d_in, const int* in_sizes, int n_in, void* d_out, int out_size,
                              void* d_ws, size_t ws_size, hipStream_t stream) {
  const u16* x = (const u16*)d_in[0];
  const u16* wsre = (const u16*)d_in[1];
  const u16* wsim = (const u16*)d_in[2];
  const u16* wdre = (const u16*)d_in[3];
  const u16* wdim = (const u16*)d_in[4];
  const u16* w1 = (const u16*)d_in[6];
  const u16* w2 = (const u16*)d_in[7];
  char* ws = (char*)d_ws + (ws_size > ((size_t)20 << 20) ? (ws_size - ((size_t)20 << 20)) & ~(size_t)0x1FFFFF : 0);
  unsigned* cnt = (unsigned*)(ws + WS_CNT);
  u16* ab = (u16*)(ws + WS_AB);
  u16* h = (u16*)(ws + WS_H);
  k1_ln_gemm1<<<256, 512, 0, stream>>>(x, wdre, wdim, w1, ab, h, cnt, wsre, wsim, w2, (const u16*)d_out);
  k2_gemm2<<<256, 512, 0, stream>>>(wsre, wsim, h, ab, w2, x, (u16*)d_out, ws + WS_P, cnt);
}
```
